```python
import torch

def _softmax(x, axis=-1):
    return torch.softmax(x, dim=axis)

def _gelu(x):
    return torch.nn.functional.gelu(x, approximate='tanh')

def _silu(x):
    return torch.nn.functional.silu(x)

def _sigmoid(x):
    return torch.sigmoid(x)

def _layernorm(x, axis=-1, eps=1e-05):
    m = x.mean(dim=axis, keepdim=True)
    v = x.var(dim=axis, keepdim=True, unbiased=False)
    return (x - m) / torch.sqrt(v + eps)

def _scatter_add(tgt, idx, src, axis=0):
    return tgt.clone().index_add_(axis, idx, src)

def _segment_sum(src, seg, n):
    out = torch.zeros((n,) + src.shape[1:], dtype=src.dtype)
    return out.index_add_(0, seg, src)

def _pad(x, pw):
    flat = tuple((v for pair in reversed(pw) for v in pair))
    return torch.nn.functional.pad(x, flat)
kernel_name = 'adaLN_cond__mha_block__glu_gated_bilinear__prompt'

def setup_inputs():

    _w = lambda a: (lambda t: t.to(torch.bfloat16) if t.is_floating_point() else t)(torch.as_tensor(a))

    def _A_setup():
        B, S, D, H = (1, 2048, 576, 32)
        return {'x': _w((lambda *s: torch.randn(*s, dtype=torch.bfloat16) * 0.05)(B, S, D)), 'wqkv': _w((lambda *s: torch.randn(*s, dtype=torch.bfloat16) * 0.05)(D, 3 * D)), 'wo': _w((lambda *s: torch.randn(*s, dtype=torch.bfloat16) * 0.05)(D, D))}
    _A_in = _A_setup()

    def _B_setup():
        B, S, D, F = (1, 2048, 576, 4608)
        return {'x': _w((lambda *s: torch.randn(*s, dtype=torch.bfloat16) * 0.05)(B, S, D)), 'w_gate': _w((lambda *s: torch.randn(*s, dtype=torch.bfloat16) * 0.8)(D, F) * D ** (-0.5)), 'w_up': _w((lambda *s: torch.randn(*s, dtype=torch.bfloat16) * 0.8)(D, F) * D ** (-0.5)), 'w_down': _w((lambda *s: torch.randn(*s, dtype=torch.bfloat16) * 0.8)(F, D) * F ** (-0.5))}
    _B_in = _B_setup()
    return {'A_x': _A_in['x'], 'A_wqkv': _A_in['wqkv'], 'A_wo': _A_in['wo'], 'B_x': _B_in['x'], 'B_w_gate': _B_in['w_gate'], 'B_w_up': _B_in['w_up'], 'B_w_down': _B_in['w_down'], 'c': _w((lambda *s: torch.randn(*s, dtype=torch.bfloat16) * 0.2)(1, 576) * 8.0), 'w_mod': _w((lambda *s: torch.randn(*s, dtype=torch.bfloat16) * 0.2)(576, 6 * 576) * 576 ** (-0.5) * 8.0)}

def reference(A_x, A_wqkv, A_wo, B_x, B_w_gate, B_w_up, B_w_down, c, w_mod):

    def _A_ref(x, wqkv, wo):
        B, S, D = x.shape
        H = 32
        Dh = D // H
        qkv = (lambda x, *d: x.permute(*d))((x @ wqkv).reshape(B, S, 3, H, Dh), 2, 0, 3, 1, 4)
        q, k, v = (qkv[0], qkv[1], qkv[2])
        scores = torch.einsum('bhqd,bhkd->bhqk', q, k) * Dh ** (-0.5)
        p = _softmax(scores, axis=-1)
        o = torch.einsum('bhqk,bhkd->bhqd', p, v)
        return (lambda x, *d: x.permute(*d))(o, 0, 2, 1, 3).reshape(B, S, D) @ wo

    def _B_ref(x, w_gate, w_up, w_down):
        g = torch.einsum('bsd,df->bsf', x, w_gate)
        u = torch.einsum('bsd,df->bsf', x, w_up)
        h = g * u
        return torch.einsum('bsf,fd->bsd', h, w_down)
    x = A_x
    mod = (_silu(c) @ w_mod).reshape(c.shape[0], 6, 1, -1)
    sa, ca, ga, sb, cb, gb = (mod[:, 0].clone(), mod[:, 1].clone(), mod[:, 2].clone(), mod[:, 3].clone(), mod[:, 4].clone(), mod[:, 5].clone())
    h = _layernorm(x, axis=-1) * (1 + ca) + sa
    x = x + ga * _A_ref(h, A_wqkv, A_wo)
    h = _layernorm(x, axis=-1) * (1 + cb) + sb
    x = x + gb * _B_ref(h, B_w_gate, B_w_up, B_w_down)
    return x
```

```cpp
#include <hip/hip_runtime.h>
#include <hip/hip_bf16.h>
#include <stdint.h>


typedef __attribute__((ext_vector_type(8))) short bf16x8;
typedef __attribute__((ext_vector_type(4))) short s16x4;
typedef __attribute__((ext_vector_type(4))) float f32x4;
typedef __attribute__((ext_vector_type(2))) float f32x2;
typedef __attribute__((ext_vector_type(16))) float f32x16;
typedef __attribute__((ext_vector_type(4))) unsigned u32x4;
typedef __attribute__((ext_vector_type(2))) unsigned u32x2;
typedef __attribute__((ext_vector_type(2))) __bf16 bf16x2_t;
typedef unsigned short u16;

#define DI __device__ __forceinline__

constexpr int S_ = 2048, D_ = 576, H_ = 32, DH_ = 18, F_ = 4608;
constexpr int HP = 32;
constexpr float QSCALE = 0.23570226039551584f * 1.4426950408889634f;

DI float bf2f(u16 v) { return __uint_as_float(((unsigned)v) << 16); }
DI float bflo(unsigned v) { return __uint_as_float(v << 16); }
DI float bfhi(unsigned v) { return __uint_as_float(v & 0xffff0000u); }
DI unsigned pack2(float a, float b) {
  f32x2 v = {a, b};
  bf16x2_t r = __builtin_convertvector(v, bf16x2_t);
  return __builtin_bit_cast(unsigned, r);
}
DI u16 f2bf(float a) { return (u16)(pack2(a, 0.f) & 0xffffu); }
DI float rbf(float a) { return bflo(pack2(a, 0.f) & 0xffffu); }

typedef __attribute__((address_space(3))) s16x4* lds_s16x4_ptr;
DI s16x4 tr_read(const char* p) {
  return __builtin_amdgcn_ds_read_tr16_b64_v4i16((lds_s16x4_ptr)(p));
}

constexpr size_t WS_MOD = 0;
constexpr size_t WS_ST1 = 16384;
constexpr size_t WS_ST2 = WS_ST1 + 2048 * 8;
constexpr size_t WS_Q = 1 << 20;
constexpr size_t WS_K = WS_Q + (size_t)H_ * S_ * HP * 2;
constexpr size_t WS_V = WS_K + (size_t)H_ * S_ * HP * 2;
constexpr size_t WS_O = WS_V + (size_t)H_ * S_ * HP * 2;
constexpr size_t WS_X1 = WS_O + (size_t)S_ * D_ * 2;
constexpr size_t WS_HH = WS_X1 + (size_t)S_ * D_ * 2;
constexpr size_t WS_PART = WS_HH + (size_t)S_ * F_ * 2;

__global__ __launch_bounds__(256) void k_prep(const u16* __restrict__ c, const u16* __restrict__ wmod,
                                               const u16* __restrict__ x, float* __restrict__ mod,
                                               float2* __restrict__ st1) {
  __shared__ float red[64][33];
  const int t = threadIdx.x;
  if (blockIdx.x < 108) {
    const int n0 = blockIdx.x * 32 + (t & 3) * 8;
    const int ks = t >> 2;
    float acc[8];
#pragma unroll
    for (int j = 0; j < 8; ++j) acc[j] = 0.f;
#pragma unroll
    for (int r = 0; r < 9; ++r) {
      const int k = ks * 9 + r;
      float cv = bf2f(c[k]);
      float sv = rbf(cv / (1.f + __expf(-cv)));
      u32x4 w = *reinterpret_cast<const u32x4*>(wmod + (size_t)k * 3456 + n0);
#pragma unroll
      for (int j = 0; j < 4; ++j) {
        acc[2 * j] += sv * bflo(w[j]);
        acc[2 * j + 1] += sv * bfhi(w[j]);
      }
    }
#pragma unroll
    for (int j = 0; j < 8; ++j) red[ks][(t & 3) * 8 + j] = acc[j];
    __syncthreads();
    if (t < 32) {
      float s = 0.f;
#pragma unroll 8
      for (int i = 0; i < 64; ++i) s += red[i][t];
      mod[blockIdx.x * 32 + t] = rbf(s);
    }
  } else {
    const int lane = t & 63, w = t >> 6;
    for (int row = (blockIdx.x - 108) * 4 + w; row < S_; row += (gridDim.x - 108) * 4) {
      const u16* xr = x + (size_t)row * D_;
      u32x4 a = *reinterpret_cast<const u32x4*>(xr + lane * 8);
      u32x4 b = {0, 0, 0, 0};
      if (lane < 8) b = *reinterpret_cast<const u32x4*>(xr + 512 + lane * 8);
      float v[16];
#pragma unroll
      for (int j = 0; j < 4; ++j) {
        v[2 * j] = bflo(a[j]); v[2 * j + 1] = bfhi(a[j]);
        v[8 + 2 * j] = bflo(b[j]); v[8 + 2 * j + 1] = bfhi(b[j]);
      }
      float s = 0.f;
#pragma unroll
      for (int j = 0; j < 16; ++j) s += v[j];
#pragma unroll
      for (int o = 32; o >= 1; o >>= 1) s += __shfl_xor(s, o);
      const float mean = s * (1.f / D_);
      float q = 0.f;
#pragma unroll
      for (int j = 0; j < 8; ++j) { float d = v[j] - mean; q += d * d; }
      if (lane < 8) {
#pragma unroll
        for (int j = 8; j < 16; ++j) { float d = v[j] - mean; q += d * d; }
      }
#pragma unroll
      for (int o = 32; o >= 1; o >>= 1) q += __shfl_xor(q, o);
      if (lane == 0) st1[row] = make_float2(mean, rsqrtf(q * (1.f / D_) + 1e-5f));
    }
  }
}

DI int a_off(int row, int chunk) { return row * 128 + ((chunk ^ ((row >> 1) & 7)) << 4); }

template <int BM, int BN, int WM, int WN, int LNMODE  , class BSrc, class Epi>
DI void gemm_body(char* lds, const u16* __restrict__ A, int lda, int m0, int kbeg, int nk,
                  const float2* __restrict__ stats, const float* __restrict__ mscale, const float* __restrict__ mshift,
                  BSrc bsrc, Epi epi) {
  constexpr int MT = BM / WM / 16, NT = BN / WN / 16;
  constexpr int ACH = BM / 32;
  constexpr int BCH = BN / 32;
  constexpr int ABYTES = BM * 128, BBYTES = 64 * BN * 2;
  const int t = threadIdx.x, lane = t & 63, wid = t >> 6;
  const int wr = wid / WN, wc = wid % WN, fr = lane & 15, fq = lane >> 4;

  float rmean[ACH], rrstd[ACH];
  if constexpr (LNMODE == 1) {
#pragma unroll
    for (int i = 0; i < ACH; ++i) { float2 s = stats[m0 + (t >> 3) + 32 * i]; rmean[i] = s.x; rrstd[i] = s.y; }
  } else if constexpr (LNMODE == 2) {
#pragma unroll
    for (int i = 0; i < ACH; ++i) {
      const float2* p = stats + (size_t)(m0 + (t >> 3) + 32 * i) * 12;
      float2 pp[12];
      float s = 0.f;
#pragma unroll
      for (int j = 0; j < 12; ++j) { pp[j] = p[j]; s += pp[j].x; }
      const float mean = s * (1.f / D_);
      float m2 = 0.f;
#pragma unroll
      for (int j = 0; j < 12; ++j) { float d = pp[j].x * (1.f / 48.f) - mean; m2 += pp[j].y + 48.f * d * d; }
      rmean[i] = mean; rrstd[i] = rsqrtf(m2 * (1.f / D_) + 1e-5f);
    }
  }

  u32x4 areg[ACH], breg[BCH];
  auto gload = [&](int kt) {
    const int k0 = kbeg + kt * 64;
#pragma unroll
    for (int i = 0; i < ACH; ++i) {
      const int row = (t >> 3) + 32 * i;
      areg[i] = *reinterpret_cast<const u32x4*>(A + (size_t)(m0 + row) * lda + k0 + (t & 7) * 8);
    }
#pragma unroll
    for (int i = 0; i < BCH; ++i) {
      const int j = t + 256 * i;
      const int kb = j / (BN), rem = j % (BN);
      const int sub = rem >> 4, r = (rem >> 1) & 7, hf = rem & 1;
      const int k = k0 + kb * 8 + (r ^ ((kb & 1) << 2));
      breg[i] = *reinterpret_cast<const u32x4*>(bsrc(k, sub) + hf * 8);
    }
  };
  auto swrite = [&](int kt, char* buf) {
    const int k0 = kbeg + kt * 64;
    if constexpr (LNMODE != 0) {
      const int kc = k0 + (t & 7) * 8;
      f32x4 sc0 = *reinterpret_cast<const f32x4*>(mscale + kc), sc1 = *reinterpret_cast<const f32x4*>(mscale + kc + 4);
      f32x4 sh0 = *reinterpret_cast<const f32x4*>(mshift + kc), sh1 = *reinterpret_cast<const f32x4*>(mshift + kc + 4);
      float sc[8] = {sc0[0] + 1.f, sc0[1] + 1.f, sc0[2] + 1.f, sc0[3] + 1.f, sc1[0] + 1.f, sc1[1] + 1.f, sc1[2] + 1.f, sc1[3] + 1.f};
      float sh[8] = {sh0[0], sh0[1], sh0[2], sh0[3], sh1[0], sh1[1], sh1[2], sh1[3]};
#pragma unroll
      for (int i = 0; i < ACH; ++i) {
        u32x4 o;
#pragma unroll
        for (int j = 0; j < 4; ++j) {
          float x0 = (bflo(areg[i][j]) - rmean[i]) * rrstd[i];
          float x1 = (bfhi(areg[i][j]) - rmean[i]) * rrstd[i];
          o[j] = pack2(x0 * sc[2 * j] + sh[2 * j], x1 * sc[2 * j + 1] + sh[2 * j + 1]);
        }
        areg[i] = o;
      }
    }
#pragma unroll
    for (int i = 0; i < ACH; ++i)
      *reinterpret_cast<u32x4*>(buf + a_off((t >> 3) + 32 * i, t & 7)) = areg[i];
#pragma unroll
    for (int i = 0; i < BCH; ++i)
      *reinterpret_cast<u32x4*>(buf + ABYTES + (t + 256 * i) * 16) = breg[i];
  };

  f32x4 acc[MT][NT];
#pragma unroll
  for (int m = 0; m < MT; ++m)
#pragma unroll
    for (int n = 0; n < NT; ++n) acc[m][n] = f32x4{0.f, 0.f, 0.f, 0.f};

  gload(0);
  swrite(0, lds);
  __syncthreads();
  const int q4 = (lane & 15) >> 2, p4 = lane & 3;
  for (int kt = 0; kt < nk; ++kt) {
    char* cur = lds + (kt & 1) * (ABYTES + BBYTES);
    char* nxt = lds + ((kt + 1) & 1) * (ABYTES + BBYTES);
    if (kt + 1 < nk) gload(kt + 1);
#pragma unroll
    for (int ks = 0; ks < 2; ++ks) {
      bf16x8 af[MT], bfr[NT];
#pragma unroll
      for (int m = 0; m < MT; ++m)
        af[m] = *reinterpret_cast<const bf16x8*>(cur + a_off(wr * (BM / WM) + m * 16 + fr, ks * 4 + fq));
      const int kb = ks * 4 + fq;
#pragma unroll
      for (int n = 0; n < NT; ++n) {
        const int sub = epi.bsub(wc, n);
        const char* base = cur + ABYTES + (kb * (BN / 16) + sub) * 256 + p4 * 8;
        const int sw = (kb & 1) << 2;
        s16x4 lo = tr_read(base + ((q4 ^ sw) << 5));
        s16x4 hi = tr_read(base + (((q4 + 4) ^ sw) << 5));
        bfr[n] = __builtin_shufflevector(lo, hi, 0, 1, 2, 3, 4, 5, 6, 7);
      }
#pragma unroll
      for (int m = 0; m < MT; ++m)
#pragma unroll
        for (int n = 0; n < NT; ++n)
          acc[m][n] = __builtin_amdgcn_mfma_f32_16x16x32_bf16(af[m], bfr[n], acc[m][n], 0, 0, 0);
    }
    if (kt + 1 < nk) swrite(kt + 1, nxt);
    __syncthreads();
  }
  epi.template run<MT, NT>(acc, m0 + wr * (BM / WM), wc, fr, fq);
}

struct EpiQKV {
  u16* qkv;
  int n0;
  DI int bsub(int wc, int n) const { return wc * 3 + n; }
  template <int MT, int NT>
  DI void run(f32x4 (&acc)[MT][NT], int row0, int wc, int fr, int fq) const {
#pragma unroll
    for (int n = 0; n < NT; ++n) {
      const int col = n0 + (wc * NT + n) * 16 + fr;
      const int which = col / D_, rem = col % D_, head = rem / DH_, d = rem % DH_;
      const float sc = which == 0 ? QSCALE : 1.f;
      u16* base = qkv + ((size_t)(which * H_ + head) * S_) * HP + d;
#pragma unroll
      for (int m = 0; m < MT; ++m)
#pragma unroll
        for (int j = 0; j < 4; ++j) {
          const int row = row0 + m * 16 + fq * 4 + j;
          u16* p = base + (size_t)row * HP;
          *p = f2bf(acc[m][n][j] * sc);
          if (d == DH_ - 1) {
            unsigned* pp = reinterpret_cast<unsigned*>(p + 1);
            pp[0] = (which == 2) ? 0x00003f80u : 0u;
#pragma unroll
            for (int z = 1; z < 7; ++z) pp[z] = 0u;
          }
        }
    }
  }
};

__global__ __launch_bounds__(256) void k_qkv(const u16* __restrict__ x, const u16* __restrict__ wqkv,
                                              const float2* __restrict__ st1, const float* __restrict__ mod,
                                              u16* __restrict__ qkv) {
  __shared__ __attribute__((aligned(16))) char lds[2 * (128 * 128 + 64 * 96 * 2)];
  const int bm = blockIdx.x & 15, bn = blockIdx.x >> 4;
  const int n0 = bn * 96;
  EpiQKV epi{qkv, n0};
  auto bsrc = [&](int k, int sub) { return wqkv + (size_t)k * (3 * D_) + n0 + sub * 16; };
  gemm_body<128, 96, 2, 2, 1>(lds, x, D_, bm * 128, 0, D_ / 64, st1, mod + D_, mod, bsrc, epi);
}

__global__ __launch_bounds__(256) void k_attn(const u16* __restrict__ Q, const u16* __restrict__ K,
                                               const u16* __restrict__ V, u16* __restrict__ O) {
  __shared__ __attribute__((aligned(16))) char lds[2 * 8192];
  const int t = threadIdx.x, lane = t & 63, wid = t >> 6, r32 = lane & 31, h = lane >> 5;
  const int head = blockIdx.x >> 4, qb = blockIdx.x & 15;
  const int q0 = qb * 128 + wid * 32;
  const u16* Qh = Q + (size_t)head * S_ * HP;
  const u16* Kh = K + (size_t)head * S_ * HP;
  const u16* Vh = V + (size_t)head * S_ * HP;
  bf16x8 qf0 = *reinterpret_cast<const bf16x8*>(Qh + (size_t)(q0 + r32) * HP + 8 * h);
  bf16x8 qf1 = *reinterpret_cast<const bf16x8*>(Qh + (size_t)(q0 + r32) * HP + 16 + 8 * h);

  const int skey = t >> 2, sch = t & 3;
  const int kst_off = skey * 64 + ((sch ^ ((skey >> 2) & 3)) << 4);
  const int vst_off = 4096 + skey * 64 + sch * 16;
  u32x4 kreg, vreg;
  auto gload = [&](int kt) {
    kreg = *reinterpret_cast<const u32x4*>(Kh + (size_t)(kt * 64 + skey) * HP + sch * 8);
    vreg = *reinterpret_cast<const u32x4*>(Vh + (size_t)(kt * 64 + skey) * HP + sch * 8);
  };
  auto swrite = [&](char* buf) {
    *reinterpret_cast<u32x4*>(buf + kst_off) = kreg;
    *reinterpret_cast<u32x4*>(buf + vst_off) = vreg;
  };

  f32x16 oacc;
#pragma unroll
  for (int i = 0; i < 16; ++i) oacc[i] = 0.f;
  float m_run = -1e30f;
  constexpr float THR = 8.f;

  const int kr0 = r32 * 64 + (((0 + h) ^ ((r32 >> 2) & 3)) << 4);
  const int kr1 = r32 * 64 + (((2 + h) ^ ((r32 >> 2) & 3)) << 4);
  const int g = lane >> 4, q4 = (lane & 15) >> 2, p4 = lane & 3;
  const int vr = 4096 + (4 * (g >> 1) + q4) * 64 + (g & 1) * 32 + p4 * 8;

  gload(0);
  swrite(lds);
  __syncthreads();
  constexpr int NT = S_ / 64;
  for (int kt = 0; kt < NT; ++kt) {
    char* cur = lds + (kt & 1) * 8192;
    char* nxt = lds + ((kt + 1) & 1) * 8192;
    if (kt + 1 < NT) gload(kt + 1);
    f32x16 s0, s1;
#pragma unroll
    for (int i = 0; i < 16; ++i) { s0[i] = 0.f; s1[i] = 0.f; }
    {
      bf16x8 ka = *reinterpret_cast<const bf16x8*>(cur + kr0);
      bf16x8 kb = *reinterpret_cast<const bf16x8*>(cur + kr1);
      bf16x8 kc = *reinterpret_cast<const bf16x8*>(cur + 2048 + kr0);
      bf16x8 kd = *reinterpret_cast<const bf16x8*>(cur + 2048 + kr1);
      s0 = __builtin_amdgcn_mfma_f32_32x32x16_bf16(ka, qf0, s0, 0, 0, 0);
      s0 = __builtin_amdgcn_mfma_f32_32x32x16_bf16(kb, qf1, s0, 0, 0, 0);
      s1 = __builtin_amdgcn_mfma_f32_32x32x16_bf16(kc, qf0, s1, 0, 0, 0);
      s1 = __builtin_amdgcn_mfma_f32_32x32x16_bf16(kd, qf1, s1, 0, 0, 0);
    }
    float mt = fmaxf(s0[0], s1[0]);
#pragma unroll
    for (int i = 1; i < 16; ++i) mt = fmaxf(mt, fmaxf(s0[i], s1[i]));
    mt = fmaxf(mt, __shfl_xor(mt, 32));
    if (!__all(mt - m_run <= THR)) {
      const float mn = fmaxf(m_run, mt);
      const float alpha = __builtin_amdgcn_exp2f(m_run - mn);
      m_run = mn;
#pragma unroll
      for (int i = 0; i < 16; ++i) oacc[i] *= alpha;
    }
    u32x4 pf[4];
#pragma unroll
    for (int i = 0; i < 8; ++i) {
      float a = __builtin_amdgcn_exp2f(s0[2 * i] - m_run), b = __builtin_amdgcn_exp2f(s0[2 * i + 1] - m_run);
      float c = __builtin_amdgcn_exp2f(s1[2 * i] - m_run), d = __builtin_amdgcn_exp2f(s1[2 * i + 1] - m_run);
      pf[i >> 2][i & 3] = pack2(a, b);
      pf[2 + (i >> 2)][i & 3] = pack2(c, d);
    }
#pragma unroll
    for (int u = 0; u < 4; ++u) {
      s16x4 lo = tr_read(cur + vr + (16 * u) * 64);
      s16x4 hi = tr_read(cur + vr + (16 * u + 8) * 64);
      bf16x8 vf = __builtin_shufflevector(lo, hi, 0, 1, 2, 3, 4, 5, 6, 7);
      oacc = __builtin_amdgcn_mfma_f32_32x32x16_bf16(vf, __builtin_bit_cast(bf16x8, pf[u]), oacc, 0, 0, 0);
    }
    if (kt + 1 < NT) swrite(nxt);
    __syncthreads();
  }
  float l = __shfl(oacc[10], r32);
  const float inv = 1.f / l;
  unsigned* op = reinterpret_cast<unsigned*>(O + (size_t)(q0 + r32) * D_ + head * DH_);
  if (h == 0) {
    op[0] = pack2(oacc[0] * inv, oacc[1] * inv);
    op[1] = pack2(oacc[2] * inv, oacc[3] * inv);
    op[4] = pack2(oacc[4] * inv, oacc[5] * inv);
    op[5] = pack2(oacc[6] * inv, oacc[7] * inv);
    op[8] = pack2(oacc[8] * inv, oacc[9] * inv);
  } else {
    op[2] = pack2(oacc[0] * inv, oacc[1] * inv);
    op[3] = pack2(oacc[2] * inv, oacc[3] * inv);
    op[6] = pack2(oacc[4] * inv, oacc[5] * inv);
    op[7] = pack2(oacc[6] * inv, oacc[7] * inv);
  }
}

struct EpiProj {
  const u16* x; const float* ga; u16* x1; float2* st2; int n0;
  DI int bsub(int wc, int n) const { return wc * 3 + n; }
  template <int MT, int NT>
  DI void run(f32x4 (&acc)[MT][NT], int row0, int wc, int fr, int fq) const {
#pragma unroll
    for (int m = 0; m < MT; ++m)
#pragma unroll
      for (int j = 0; j < 4; ++j) {
        const int row = row0 + m * 16 + fq * 4 + j;
        float v[NT];
        float s = 0.f;
#pragma unroll
        for (int n = 0; n < NT; ++n) {
          const int col = n0 + (wc * NT + n) * 16 + fr;
          float r = bf2f(x[(size_t)row * D_ + col]) + ga[col] * acc[m][n][j];
          u16 rb = f2bf(r);
          x1[(size_t)row * D_ + col] = rb;
          v[n] = bf2f(rb);
          s += v[n];
        }
#pragma unroll
        for (int o = 8; o >= 1; o >>= 1) s += __shfl_xor(s, o);
        const float lm = s * (1.f / (16 * NT));
        float q = 0.f;
#pragma unroll
        for (int n = 0; n < NT; ++n) { float d = v[n] - lm; q += d * d; }
#pragma unroll
        for (int o = 8; o >= 1; o >>= 1) q += __shfl_xor(q, o);
        if (fr == 0) st2[(size_t)row * 12 + (n0 / 48 + wc)] = make_float2(s, q);
      }
  }
};

__global__ __launch_bounds__(256) void k_proj(const u16* __restrict__ o, const u16* __restrict__ wo,
                                               const u16* __restrict__ x, const float* __restrict__ mod,
                                               u16* __restrict__ x1, float2* __restrict__ st2) {
  __shared__ __attribute__((aligned(16))) char lds[2 * (64 * 128 + 64 * 96 * 2)];
  const int bm = blockIdx.x & 31, bn = blockIdx.x >> 5;
  const int n0 = bn * 96;
  EpiProj epi{x, mod + 2 * D_, x1, st2, n0};
  auto bsrc = [&](int k, int sub) { return wo + (size_t)k * D_ + n0 + sub * 16; };
  gemm_body<64, 96, 2, 2, 0>(lds, o, D_, bm * 64, 0, D_ / 64, nullptr, nullptr, nullptr, bsrc, epi);
}

struct EpiGLU {
  u16* hh; int n0;
  DI int bsub(int wc, int n) const { return (n >> 1) * 4 + wc * 2 + (n & 1); }
  template <int MT, int NT>
  DI void run(f32x4 (&acc)[MT][NT], int row0, int wc, int fr, int fq) const {
#pragma unroll
    for (int m = 0; m < MT; ++m)
#pragma unroll
      for (int n = 0; n < 2; ++n) {
        const int col = n0 + (wc * 2 + n) * 16 + fr;
#pragma unroll
        for (int j = 0; j < 4; ++j) {
          const int row = row0 + m * 16 + fq * 4 + j;
          hh[(size_t)row * F_ + col] = f2bf(acc[m][n][j] * acc[m][n + 2][j]);
        }
      }
  }
};

__global__ __launch_bounds__(256) void k_glu(const u16* __restrict__ x1, const u16* __restrict__ wg,
                                              const u16* __restrict__ wu, const float2* __restrict__ st2,
                                              const float* __restrict__ mod, u16* __restrict__ hh) {
  __shared__ __attribute__((aligned(16))) char lds[2 * (128 * 128 + 64 * 128 * 2)];
  const int bm = blockIdx.x & 15, bn = blockIdx.x >> 4;
  const int n0 = bn * 64;
  EpiGLU epi{hh, n0};
  auto bsrc = [&](int k, int sub) { return (sub < 4 ? wg : wu) + (size_t)k * F_ + n0 + (sub & 3) * 16; };
  gemm_body<128, 128, 2, 2, 2>(lds, x1, D_, bm * 128, 0, D_ / 64, st2, mod + 4 * D_, mod + 3 * D_, bsrc, epi);
}

struct EpiPart {
  float* part; int n0;
  DI int bsub(int wc, int n) const { return wc * 3 + n; }
  template <int MT, int NT>
  DI void run(f32x4 (&acc)[MT][NT], int row0, int wc, int fr, int fq) const {
#pragma unroll
    for (int m = 0; m < MT; ++m)
#pragma unroll
      for (int n = 0; n < NT; ++n) {
        const int col = n0 + (wc * NT + n) * 16 + fr;
#pragma unroll
        for (int j = 0; j < 4; ++j) {
          const int row = row0 + m * 16 + fq * 4 + j;
          part[(size_t)row * D_ + col] = acc[m][n][j];
        }
      }
  }
};

__global__ __launch_bounds__(256) void k_down(const u16* __restrict__ hh, const u16* __restrict__ wd,
                                               float* __restrict__ part) {
  __shared__ __attribute__((aligned(16))) char lds[2 * (128 * 128 + 64 * 96 * 2)];
  const int bm = blockIdx.x & 15, rest = blockIdx.x >> 4;
  const int bn = rest % 6, sk = rest / 6;
  const int n0 = bn * 96;
  EpiPart epi{part + (size_t)sk * S_ * D_, n0};
  auto bsrc = [&](int k, int sub) { return wd + (size_t)k * D_ + n0 + sub * 16; };
  gemm_body<128, 96, 2, 2, 0>(lds, hh, F_, bm * 128, sk * (F_ / 3), F_ / 3 / 64, nullptr, nullptr, nullptr, bsrc, epi);
}

__global__ __launch_bounds__(256) void k_final(const float* __restrict__ part, const u16* __restrict__ x1,
                                                const float* __restrict__ mod, u16* __restrict__ out) {
  const int i = (blockIdx.x * 256 + threadIdx.x) * 8;
  if (i >= S_ * D_) return;
  const int col = i % D_;
  const float* gb = mod + 5 * D_ + col;
  u32x4 xv = *reinterpret_cast<const u32x4*>(x1 + i);
  u32x4 o;
#pragma unroll
  for (int j = 0; j < 4; ++j) {
    float a = 0.f, b = 0.f;
#pragma unroll
    for (int s = 0; s < 3; ++s) {
      f32x2 p = *reinterpret_cast<const f32x2*>(part + (size_t)s * S_ * D_ + i + 2 * j);
      a += p[0]; b += p[1];
    }
    o[j] = pack2(bflo(xv[j]) + gb[2 * j] * a, bfhi(xv[j]) + gb[2 * j + 1] * b);
  }
  *reinterpret_cast<u32x4*>(out + i) = o;
}

extern "C" void kernel_launch(void* const* d_in, const int* in_sizes, int n_in,
                              void* d_out, int out_size, void* d_ws, size_t ws_size,
                              hipStream_t stream) {
  const u16* x = (const u16*)d_in[0];
  const u16* wqkv = (const u16*)d_in[1];
  const u16* wo = (const u16*)d_in[2];
  const u16* wg = (const u16*)d_in[4];
  const u16* wu = (const u16*)d_in[5];
  const u16* wd = (const u16*)d_in[6];
  const u16* c = (const u16*)d_in[7];
  const u16* wmod = (const u16*)d_in[8];
  char* ws = (char*)d_ws;
  float* mod = (float*)(ws + WS_MOD);
  float2* st1 = (float2*)(ws + WS_ST1);
  float2* st2 = (float2*)(ws + WS_ST2);
  u16* Q = (u16*)(ws + WS_Q);
  u16* K = (u16*)(ws + WS_K);
  u16* V = (u16*)(ws + WS_V);
  u16* O = (u16*)(ws + WS_O);
  u16* x1 = (u16*)(ws + WS_X1);
  u16* hh = (u16*)(ws + WS_HH);
  float* part = (float*)(ws + WS_PART);

  k_prep<<<108 + 128, 256, 0, stream>>>(c, wmod, x, mod, st1);
  k_qkv<<<16 * 18, 256, 0, stream>>>(x, wqkv, st1, mod, Q);
  k_attn<<<32 * 16, 256, 0, stream>>>(Q, K, V, O);
  k_proj<<<32 * 6, 256, 0, stream>>>(O, wo, x, mod, x1, st2);
  k_glu<<<16 * 72, 256, 0, stream>>>(x1, wg, wu, st2, mod, hh);
  k_down<<<16 * 6 * 3, 256, 0, stream>>>(hh, wd, part);
  k_final<<<(S_ * D_ / 8 + 255) / 256, 256, 0, stream>>>(part, x1, mod, (u16*)d_out);
}
```

```cpp
#include <hip/hip_runtime.h>
#include <hip/hip_bf16.h>
#include <stdint.h>


typedef __attribute__((ext_vector_type(8))) short bf16x8;
typedef __attribute__((ext_vector_type(4))) short s16x4;
typedef __attribute__((ext_vector_type(4))) float f32x4;
typedef __attribute__((ext_vector_type(2))) float f32x2;
typedef __attribute__((ext_vector_type(16))) float f32x16;
typedef __attribute__((ext_vector_type(4))) unsigned u32x4;
typedef __attribute__((ext_vector_type(2))) unsigned u32x2;
typedef __attribute__((ext_vector_type(2))) __bf16 bf16x2_t;
typedef unsigned short u16;

#define DI __device__ __forceinline__

constexpr int S_ = 2048, D_ = 576, H_ = 32, DH_ = 18, F_ = 4608;
constexpr int HP = 32;
constexpr float QSCALE = 0.23570226039551584f * 1.4426950408889634f;

DI float bf2f(u16 v) { return __uint_as_float(((unsigned)v) << 16); }
DI float bflo(unsigned v) { return __uint_as_float(v << 16); }
DI float bfhi(unsigned v) { return __uint_as_float(v & 0xffff0000u); }
DI unsigned pack2(float a, float b) {
  f32x2 v = {a, b};
  bf16x2_t r = __builtin_convertvector(v, bf16x2_t);
  return __builtin_bit_cast(unsigned, r);
}
DI u16 f2bf(float a) { return (u16)(pack2(a, 0.f) & 0xffffu); }
DI float rbf(float a) { return bflo(pack2(a, 0.f) & 0xffffu); }

typedef __attribute__((address_space(3))) s16x4* lds_s16x4_ptr;
DI s16x4 tr_read(const char* p) {
  return __builtin_amdgcn_ds_read_tr16_b64_v4i16((lds_s16x4_ptr)(p));
}

constexpr size_t WS_MOD = 0;
constexpr size_t WS_ST1 = 16384;
constexpr size_t WS_ST2 = WS_ST1 + 2048 * 8;
constexpr size_t WS_Q = 1 << 20;
constexpr size_t WS_K = WS_Q + (size_t)H_ * S_ * HP * 2;
constexpr size_t WS_V = WS_K + (size_t)H_ * S_ * HP * 2;
constexpr size_t WS_O = WS_V + (size_t)H_ * S_ * HP * 2;
constexpr size_t WS_X1 = WS_O + (size_t)S_ * D_ * 2;
constexpr size_t WS_HH = WS_X1 + (size_t)S_ * D_ * 2;
constexpr size_t WS_PART = WS_HH + (size_t)S_ * F_ * 2;

__global__ __launch_bounds__(256) void k_prep(const u16* __restrict__ c, const u16* __restrict__ wmod,
                                               const u16* __restrict__ x, float* __restrict__ mod,
                                               float2* __restrict__ st1) {
  __shared__ float red[64][33];
  const int t = threadIdx.x;
  if (blockIdx.x < 108) {
    const int n0 = blockIdx.x * 32 + (t & 3) * 8;
    const int ks = t >> 2;
    float acc[8];
#pragma unroll
    for (int j = 0; j < 8; ++j) acc[j] = 0.f;
#pragma unroll
    for (int r = 0; r < 9; ++r) {
      const int k = ks * 9 + r;
      float cv = bf2f(c[k]);
      float sv = rbf(cv / (1.f + __expf(-cv)));
      u32x4 w = *reinterpret_cast<const u32x4*>(wmod + (size_t)k * 3456 + n0);
#pragma unroll
      for (int j = 0; j < 4; ++j) {
        acc[2 * j] += sv * bflo(w[j]);
        acc[2 * j + 1] += sv * bfhi(w[j]);
      }
    }
#pragma unroll
    for (int j = 0; j < 8; ++j) red[ks][(t & 3) * 8 + j] = acc[j];
    __syncthreads();
    if (t < 32) {
      float s = 0.f;
#pragma unroll 8
      for (int i = 0; i < 64; ++i) s += red[i][t];
      mod[blockIdx.x * 32 + t] = rbf(s);
    }
  } else {
    const int lane = t & 63, w = t >> 6;
    for (int row = (blockIdx.x - 108) * 4 + w; row < S_; row += (gridDim.x - 108) * 4) {
      const u16* xr = x + (size_t)row * D_;
      u32x4 a = *reinterpret_cast<const u32x4*>(xr + lane * 8);
      u32x4 b = {0, 0, 0, 0};
      if (lane < 8) b = *reinterpret_cast<const u32x4*>(xr + 512 + lane * 8);
      float v[16];
#pragma unroll
      for (int j = 0; j < 4; ++j) {
        v[2 * j] = bflo(a[j]); v[2 * j + 1] = bfhi(a[j]);
        v[8 + 2 * j] = bflo(b[j]); v[8 + 2 * j + 1] = bfhi(b[j]);
      }
      float s = 0.f;
#pragma unroll
      for (int j = 0; j < 16; ++j) s += v[j];
#pragma unroll
      for (int o = 32; o >= 1; o >>= 1) s += __shfl_xor(s, o);
      const float mean = s * (1.f / D_);
      float q = 0.f;
#pragma unroll
      for (int j = 0; j < 8; ++j) { float d = v[j] - mean; q += d * d; }
      if (lane < 8) {
#pragma unroll
        for (int j = 8; j < 16; ++j) { float d = v[j] - mean; q += d * d; }
      }
#pragma unroll
      for (int o = 32; o >= 1; o >>= 1) q += __shfl_xor(q, o);
      if (lane == 0) st1[row] = make_float2(mean, rsqrtf(q * (1.f / D_) + 1e-5f));
    }
  }
}

#define WAIT_VM0() asm volatile("s_waitcnt vmcnt(0)" ::: "memory")
#define WAIT_LGKM0() asm volatile("s_waitcnt lgkmcnt(0)" ::: "memory")
#define RAW_BAR() __builtin_amdgcn_s_barrier()

template <int N> DI void wait_vm() { asm volatile("s_waitcnt vmcnt(%0)" ::"n"(N) : "memory"); }

template <int BK> DI int a_swz(int row) { return BK == 64 ? ((row >> 1) & 7) : ((-(row >> 2)) & 3); }

template <int BM, int BN, int NIMG, int WM, int WK, int BK, int NST, int LNMODE  , class Epi>
DI void gemm_body(char* lds, const u16* __restrict__ A, int lda, int m0, int kbeg, int nk,
                  const float2* __restrict__ stats, const float* __restrict__ mscale, const float* __restrict__ mshift,
                  const u16* __restrict__ B0, const u16* __restrict__ B1, int ldb, Epi& epi) {
  constexpr int NTHR = 64 * WM * WK, NW = WM * WK;
  constexpr int NT = BN / 16, BNI = BN / NIMG, NTI = NT / NIMG;
  constexpr int ABYTES = BM * BK * 2, BBYTES = BK * BN * 2, STAGE = ABYTES + BBYTES;
  constexpr int AINS = ABYTES / 1024, BINS = BBYTES / 1024;
  constexpr int BINS_I = BINS / NIMG;
  constexpr int TINS = AINS + BINS;
  constexpr int G = (TINS + NW - 1) / NW;
  constexpr int SUB = BK / 32 / WK;
  constexpr int CPR = BK / 8;
  static_assert(BBYTES % (1024 * NIMG) == 0 && ABYTES % 1024 == 0 && (BK == 32 || BK == 64), "tile");
  const int t = threadIdx.x, lane = t & 63;
  const int wid = __builtin_amdgcn_readfirstlane(t >> 6);
  const int wr = wid % WM, wk = wid / WM, fr = lane & 15, fq = lane >> 4;
  float* lnp = reinterpret_cast<float*>(lds + NST * STAGE);

  unsigned goff[G];
#pragma unroll
  for (int ii = 0; ii < G; ++ii) {
    int i = wid + ii * NW;
    if (i >= TINS) i -= TINS;
    if (i < AINS) {
      const int j = i * 64 + lane;
      const int row = j / CPR, pos = j % CPR;
      goff[ii] = (unsigned)((row * lda + (pos ^ a_swz<BK>(row)) * 8) * 2);
    } else {
      const int j = ((i - AINS) % BINS_I) * 64 + lane;
      const int kb = j / BNI, rem = j % BNI;
      const int sub = rem >> 4, r = (rem >> 1) & 7, hf = rem & 1;
      goff[ii] = (unsigned)(((kb * 8 + (r ^ ((kb & 1) << 2))) * ldb + sub * 16 + hf * 8) * 2);
    }
  }
  const char* Abase = (const char*)(A + (size_t)m0 * lda + kbeg);
  const char* B0base = (const char*)(B0 + (size_t)kbeg * ldb);
  const char* B1base = (const char*)(B1 + (size_t)kbeg * ldb);

  auto stage_issue = [&](int kt) {
    char* buf = lds + (kt % NST) * STAGE;
    const size_t bk = (size_t)kt * BK * ldb * 2;
    const char* ab = Abase + kt * BK * 2;
#pragma unroll
    for (int ii = 0; ii < G; ++ii) {
      int i = wid + ii * NW;
      if (i >= TINS) i -= TINS;
      const char* base = (i < AINS) ? ab : (((NIMG == 2 && i - AINS >= BINS_I) ? B1base : B0base) + bk);
      __builtin_amdgcn_global_load_lds((const unsigned*)(base + goff[ii]),
                                       (__attribute__((address_space(3))) unsigned*)(buf + (i * 64 + lane) * 16), 16, 0, 0);
    }
  };

#pragma unroll
  for (int st = 0; st < NST - 1; ++st)
    if (st < nk) stage_issue(st);

  float ra[2], rb[2];
  if constexpr (LNMODE != 0) {
    for (int i = t; i < D_; i += NTHR) { lnp[i] = mscale[i] + 1.f; lnp[D_ + i] = mshift[i]; }
#pragma unroll
    for (int m = 0; m < 2; ++m) {
      const int row = m0 + wr * 32 + m * 16 + fr;
      float mean, rstd;
      if constexpr (LNMODE == 1) { float2 s = stats[row]; mean = s.x; rstd = s.y; }
      else {
        const float2* p = stats + (size_t)row * 4;
        float2 p0 = p[0], p1 = p[1], p2 = p[2], p3 = p[3];
        mean = (p0.x + p1.x + p2.x + p3.x) * (1.f / D_);
        float d0 = p0.x * (1.f / 144.f) - mean, d1 = p1.x * (1.f / 144.f) - mean, d2 = p2.x * (1.f / 144.f) - mean, d3 = p3.x * (1.f / 144.f) - mean;
        float m2 = p0.y + p1.y + p2.y + p3.y + 144.f * (d0 * d0 + d1 * d1 + d2 * d2 + d3 * d3);
        rstd = rsqrtf(m2 * (1.f / D_) + 1e-5f);
      }
      ra[m] = rstd; rb[m] = -mean * rstd;
    }
    wait_vm<0>();
    WAIT_LGKM0();
  }

  f32x4 acc[2][NT];
#pragma unroll
  for (int m = 0; m < 2; ++m)
#pragma unroll
    for (int n = 0; n < NT; ++n) acc[m][n] = f32x4{0.f, 0.f, 0.f, 0.f};

  const int q4 = fr >> 2, p4 = lane & 3;
  const int arow0 = wr * 32 + fr, arow1 = arow0 + 16;
  for (int kt = 0; kt < nk; ++kt) {
    if (kt + NST - 2 < nk) wait_vm<G * (NST - 2)>(); else wait_vm<0>();
    RAW_BAR();
    if (kt + NST - 1 < nk) stage_issue(kt + NST - 1);
    const char* cur = lds + (kt % NST) * STAGE;
    {
      constexpr int PF = 4;
      auto rda = [&](int s2, int m) -> bf16x8 {
        const int ss = wk + s2 * WK;
        const int ch = ss * 4 + fq;
        const int ar = m ? arow1 : arow0;
        bf16x8 v = *reinterpret_cast<const bf16x8*>(cur + ar * (BK * 2) + ((ch ^ a_swz<BK>(ar)) << 4));
        if constexpr (LNMODE != 0) {
          const int kc = kbeg + kt * BK + ss * 32 + fq * 8;
          f32x4 sc0 = *reinterpret_cast<const f32x4*>(lnp + kc), sc1 = *reinterpret_cast<const f32x4*>(lnp + kc + 4);
          f32x4 sh0 = *reinterpret_cast<const f32x4*>(lnp + D_ + kc), sh1 = *reinterpret_cast<const f32x4*>(lnp + D_ + kc + 4);
          u32x4 r = __builtin_bit_cast(u32x4, v), o;
          const float a = ra[m], b = rb[m];
          o[0] = pack2((bflo(r[0]) * a + b) * sc0[0] + sh0[0], (bfhi(r[0]) * a + b) * sc0[1] + sh0[1]);
          o[1] = pack2((bflo(r[1]) * a + b) * sc0[2] + sh0[2], (bfhi(r[1]) * a + b) * sc0[3] + sh0[3]);
          o[2] = pack2((bflo(r[2]) * a + b) * sc1[0] + sh1[0], (bfhi(r[2]) * a + b) * sc1[1] + sh1[1]);
          o[3] = pack2((bflo(r[3]) * a + b) * sc1[2] + sh1[2], (bfhi(r[3]) * a + b) * sc1[3] + sh1[3]);
          v = __builtin_bit_cast(bf16x8, o);
        }
        return v;
      };
      auto rdb = [&](int idx) -> bf16x8 {
        const int s2 = idx / NT, n = idx % NT;
        const int ss = wk + s2 * WK;
        const int kb = ss * 4 + fq;
        const int sw = (kb & 1) << 2;
        const char* bb = cur + ABYTES + (n / NTI) * (BBYTES / NIMG) + kb * (BNI * 16) + p4 * 8 + (n % NTI) * 256;
        s16x4 lo = tr_read(bb + ((q4 ^ sw) << 5));
        s16x4 hi = tr_read(bb + (((q4 + 4) ^ sw) << 5));
        return __builtin_shufflevector(lo, hi, 0, 1, 2, 3, 4, 5, 6, 7);
      };
      bf16x8 bq[PF];
      bf16x8 a0 = rda(0, 0), a1 = rda(0, 1), a0n, a1n;
#pragma unroll
      for (int i = 0; i < PF - 1; ++i) bq[i] = rdb(i);
#pragma unroll
      for (int idx = 0; idx < SUB * NT; ++idx) {
        const int s2 = idx / NT, n = idx % NT;
        if (idx + PF - 1 < SUB * NT) bq[(idx + PF - 1) % PF] = rdb(idx + PF - 1);
        if (n == NT - PF && s2 + 1 < SUB) { a0n = rda(s2 + 1, 0); a1n = rda(s2 + 1, 1); }
        acc[0][n] = __builtin_amdgcn_mfma_f32_16x16x32_bf16(bq[idx % PF], a0, acc[0][n], 0, 0, 0);
        acc[1][n] = __builtin_amdgcn_mfma_f32_16x16x32_bf16(bq[idx % PF], a1, acc[1][n], 0, 0, 0);
        if (n == NT - 1 && s2 + 1 < SUB) { a0 = a0n; a1 = a1n; }
      }
    }
    WAIT_LGKM0();
  }
  if constexpr (WK > 1) __syncthreads();
  if constexpr (WK > 1) {
    f32x4* red = reinterpret_cast<f32x4*>(lds);
    if (wk > 0) {
#pragma unroll
      for (int m = 0; m < 2; ++m)
#pragma unroll
        for (int n = 0; n < NT; ++n) red[(((wk - 1) * WM + wr) * 2 * NT + m * NT + n) * 64 + lane] = acc[m][n];
    }
    __syncthreads();
    if (wk > 0) return;
#pragma unroll
    for (int g = 1; g < WK; ++g)
#pragma unroll
      for (int m = 0; m < 2; ++m)
#pragma unroll
        for (int n = 0; n < NT; ++n) acc[m][n] += red[(((g - 1) * WM + wr) * 2 * NT + m * NT + n) * 64 + lane];
  }
  epi.template run<NT>(acc, m0 + wr * 32 + fr, fq);
}

struct EpiQKV {
  u16* qkv;
  int n0;
  template <int NT>
  DI void run(f32x4 (&acc)[2][NT], int row0, int fq) const {
#pragma unroll
    for (int n = 0; n < NT; ++n) {
#pragma unroll
      for (int jp = 0; jp < 2; ++jp) {
        const int col = n0 + n * 16 + fq * 4 + jp * 2;
        const int which = col / D_, rem = col % D_, head = rem / DH_, d = rem % DH_;
        const float sc = which == 0 ? QSCALE : 1.f;
        u16* base = qkv + ((size_t)(which * H_ + head) * S_) * HP + d;
#pragma unroll
        for (int m = 0; m < 2; ++m) {
          unsigned* p = reinterpret_cast<unsigned*>(base + (size_t)(row0 + m * 16) * HP);
          p[0] = pack2(acc[m][n][jp * 2] * sc, acc[m][n][jp * 2 + 1] * sc);
          if (d == DH_ - 2) {
            p[1] = (which == 2) ? 0x00003f80u : 0u;
#pragma unroll
            for (int z = 2; z < 8; ++z) p[z] = 0u;
          }
        }
      }
    }
  }
};

constexpr int LDS_QKV = 4 * (128 * 64 * 2 + 64 * 144 * 2) + 2 * D_ * 4;
__global__ __launch_bounds__(512) void k_qkv(const u16* __restrict__ x, const u16* __restrict__ wqkv,
                                              const float2* __restrict__ st1, const float* __restrict__ mod,
                                              u16* __restrict__ qkv) {
  extern __shared__ __attribute__((aligned(16))) char lds[];
  const int b = blockIdx.x;
  const int xcd = b & 7, idx = b >> 3;
  const int bn = (idx % 3) * 4 + (xcd >> 1), bm = (idx / 3) * 2 + (xcd & 1);
  const int n0 = bn * 144;
  EpiQKV epi{qkv, n0};
  gemm_body<128, 144, 1, 4, 2, 64, 4, 1>(lds, x, D_, bm * 128, 0, D_ / 64, st1, mod + D_, mod, wqkv + n0, wqkv + n0, 3 * D_, epi);
}

__global__ __launch_bounds__(256) void k_attn(const u16* __restrict__ Q, const u16* __restrict__ K,
                                               const u16* __restrict__ V, u16* __restrict__ O) {
  __shared__ __attribute__((aligned(16))) char lds[2 * 8192];
  const int t = threadIdx.x, lane = t & 63, wid = t >> 6, r32 = lane & 31, h = lane >> 5;
  const int head = blockIdx.x >> 4, qb = blockIdx.x & 15;
  const int q0 = qb * 128 + wid * 32;
  const u16* Qh = Q + (size_t)head * S_ * HP;
  const u16* Kh = K + (size_t)head * S_ * HP;
  const u16* Vh = V + (size_t)head * S_ * HP;
  bf16x8 qf0 = *reinterpret_cast<const bf16x8*>(Qh + (size_t)(q0 + r32) * HP + 8 * h);
  bf16x8 qf1 = *reinterpret_cast<const bf16x8*>(Qh + (size_t)(q0 + r32) * HP + 16 + 8 * h);

  const int skey = t >> 2, sch = t & 3;
  const int kst_off = skey * 64 + ((sch ^ ((skey >> 2) & 3)) << 4);
  const int vst_off = 4096 + skey * 64 + sch * 16;
  u32x4 kreg, vreg;
  auto gload = [&](int kt) {
    kreg = *reinterpret_cast<const u32x4*>(Kh + (size_t)(kt * 64 + skey) * HP + sch * 8);
    vreg = *reinterpret_cast<const u32x4*>(Vh + (size_t)(kt * 64 + skey) * HP + sch * 8);
  };
  auto swrite = [&](char* buf) {
    *reinterpret_cast<u32x4*>(buf + kst_off) = kreg;
    *reinterpret_cast<u32x4*>(buf + vst_off) = vreg;
  };

  f32x16 oacc;
#pragma unroll
  for (int i = 0; i < 16; ++i) oacc[i] = 0.f;
  float m_run = -1e30f;
  constexpr float THR = 8.f;

  const int kr0 = r32 * 64 + (((0 + h) ^ ((r32 >> 2) & 3)) << 4);
  const int kr1 = r32 * 64 + (((2 + h) ^ ((r32 >> 2) & 3)) << 4);
  const int g = lane >> 4, q4 = (lane & 15) >> 2, p4 = lane & 3;
  const int vr = 4096 + (4 * (g >> 1) + q4) * 64 + (g & 1) * 32 + p4 * 8;

  gload(0);
  swrite(lds);
  __syncthreads();
  constexpr int NT = S_ / 64;
  for (int kt = 0; kt < NT; ++kt) {
    char* cur = lds + (kt & 1) * 8192;
    char* nxt = lds + ((kt + 1) & 1) * 8192;
    if (kt + 1 < NT) gload(kt + 1);
    f32x16 s0, s1;
#pragma unroll
    for (int i = 0; i < 16; ++i) { s0[i] = 0.f; s1[i] = 0.f; }
    {
      bf16x8 ka = *reinterpret_cast<const bf16x8*>(cur + kr0);
      bf16x8 kb = *reinterpret_cast<const bf16x8*>(cur + kr1);
      bf16x8 kc = *reinterpret_cast<const bf16x8*>(cur + 2048 + kr0);
      bf16x8 kd = *reinterpret_cast<const bf16x8*>(cur + 2048 + kr1);
      s0 = __builtin_amdgcn_mfma_f32_32x32x16_bf16(ka, qf0, s0, 0, 0, 0);
      s0 = __builtin_amdgcn_mfma_f32_32x32x16_bf16(kb, qf1, s0, 0, 0, 0);
      s1 = __builtin_amdgcn_mfma_f32_32x32x16_bf16(kc, qf0, s1, 0, 0, 0);
      s1 = __builtin_amdgcn_mfma_f32_32x32x16_bf16(kd, qf1, s1, 0, 0, 0);
    }
    float mt = fmaxf(s0[0], s1[0]);
#pragma unroll
    for (int i = 1; i < 16; ++i) mt = fmaxf(mt, fmaxf(s0[i], s1[i]));
    mt = fmaxf(mt, __shfl_xor(mt, 32));
    if (!__all(mt - m_run <= THR)) {
      const float mn = fmaxf(m_run, mt);
      const float alpha = __builtin_amdgcn_exp2f(m_run - mn);
      m_run = mn;
#pragma unroll
      for (int i = 0; i < 16; ++i) oacc[i] *= alpha;
    }
    u32x4 pf[4];
#pragma unroll
    for (int i = 0; i < 8; ++i) {
      float a = __builtin_amdgcn_exp2f(s0[2 * i] - m_run), b = __builtin_amdgcn_exp2f(s0[2 * i + 1] - m_run);
      float c = __builtin_amdgcn_exp2f(s1[2 * i] - m_run), d = __builtin_amdgcn_exp2f(s1[2 * i + 1] - m_run);
      pf[i >> 2][i & 3] = pack2(a, b);
      pf[2 + (i >> 2)][i & 3] = pack2(c, d);
    }
#pragma unroll
    for (int u = 0; u < 4; ++u) {
      s16x4 lo = tr_read(cur + vr + (16 * u) * 64);
      s16x4 hi = tr_read(cur + vr + (16 * u + 8) * 64);
      bf16x8 vf = __builtin_shufflevector(lo, hi, 0, 1, 2, 3, 4, 5, 6, 7);
      oacc = __builtin_amdgcn_mfma_f32_32x32x16_bf16(vf, __builtin_bit_cast(bf16x8, pf[u]), oacc, 0, 0, 0);
    }
    if (kt + 1 < NT) swrite(nxt);
    __syncthreads();
  }
  float l = __shfl(oacc[10], r32);
  const float inv = 1.f / l;
  unsigned* op = reinterpret_cast<unsigned*>(O + (size_t)(q0 + r32) * D_ + head * DH_);
  if (h == 0) {
    op[0] = pack2(oacc[0] * inv, oacc[1] * inv);
    op[1] = pack2(oacc[2] * inv, oacc[3] * inv);
    op[4] = pack2(oacc[4] * inv, oacc[5] * inv);
    op[5] = pack2(oacc[6] * inv, oacc[7] * inv);
    op[8] = pack2(oacc[8] * inv, oacc[9] * inv);
  } else {
    op[2] = pack2(oacc[0] * inv, oacc[1] * inv);
    op[3] = pack2(oacc[2] * inv, oacc[3] * inv);
    op[6] = pack2(oacc[4] * inv, oacc[5] * inv);
    op[7] = pack2(oacc[6] * inv, oacc[7] * inv);
  }
}

struct EpiProj {
  const u16* x; const float* ga; u16* x1; float2* st2; int n0;
  template <int NT>
  DI void run(f32x4 (&acc)[2][NT], int row0, int fq) const {
#pragma unroll
    for (int m = 0; m < 2; ++m) {
      const int row = row0 + m * 16;
      float v[NT][4];
      float s = 0.f;
#pragma unroll
      for (int n = 0; n < NT; ++n) {
        const int col = n0 + n * 16 + fq * 4;
        u32x2 xv = *reinterpret_cast<const u32x2*>(x + (size_t)row * D_ + col);
        f32x4 g = *reinterpret_cast<const f32x4*>(ga + col);
        u32x2 o;
        o[0] = pack2(bflo(xv[0]) + g[0] * acc[m][n][0], bfhi(xv[0]) + g[1] * acc[m][n][1]);
        o[1] = pack2(bflo(xv[1]) + g[2] * acc[m][n][2], bfhi(xv[1]) + g[3] * acc[m][n][3]);
        *reinterpret_cast<u32x2*>(x1 + (size_t)row * D_ + col) = o;
        v[n][0] = bflo(o[0]); v[n][1] = bfhi(o[0]); v[n][2] = bflo(o[1]); v[n][3] = bfhi(o[1]);
        s += (v[n][0] + v[n][1]) + (v[n][2] + v[n][3]);
      }
      s += __shfl_xor(s, 16); s += __shfl_xor(s, 32);
      const float lm = s * (1.f / (16 * NT));
      float q = 0.f;
#pragma unroll
      for (int n = 0; n < NT; ++n)
#pragma unroll
        for (int j = 0; j < 4; ++j) { float d = v[n][j] - lm; q += d * d; }
      q += __shfl_xor(q, 16); q += __shfl_xor(q, 32);
      if (fq == 0) st2[(size_t)row * 4 + n0 / 144] = make_float2(s, q);
    }
  }
};

constexpr int LDS_PROJ = 4 * (32 * 64 * 2 + 64 * 144 * 2);
__global__ __launch_bounds__(128) void k_proj(const u16* __restrict__ o, const u16* __restrict__ wo,
                                               const u16* __restrict__ x, const float* __restrict__ mod,
                                               u16* __restrict__ x1, float2* __restrict__ st2) {
  extern __shared__ __attribute__((aligned(16))) char lds[];
  const int b = blockIdx.x;
  const int bn = b & 3, bm = b >> 2;
  const int n0 = bn * 144;
  EpiProj epi{x, mod + 2 * D_, x1, st2, n0};
  gemm_body<32, 144, 1, 1, 2, 64, 4, 0>(lds, o, D_, bm * 32, 0, D_ / 64, nullptr, nullptr, nullptr, wo + n0, wo + n0, D_, epi);
}

struct EpiGLU {
  u16* hh; int n0;
  template <int NT>
  DI void run(f32x4 (&acc)[2][NT], int row0, int fq) const {
#pragma unroll
    for (int m = 0; m < 2; ++m)
#pragma unroll
      for (int n = 0; n < NT / 2; ++n) {
        u32x2 o;
        o[0] = pack2(acc[m][n][0] * acc[m][n + NT / 2][0], acc[m][n][1] * acc[m][n + NT / 2][1]);
        o[1] = pack2(acc[m][n][2] * acc[m][n + NT / 2][2], acc[m][n][3] * acc[m][n + NT / 2][3]);
        *reinterpret_cast<u32x2*>(hh + (size_t)(row0 + m * 16) * F_ + n0 + n * 16 + fq * 4) = o;
      }
  }
};

constexpr int LDS_GLU = 4 * (256 * 32 * 2 + 32 * 288 * 2) + 2 * D_ * 4;
__global__ __launch_bounds__(512) void k_glu(const u16* __restrict__ x1, const u16* __restrict__ wg,
                                              const u16* __restrict__ wu, const float2* __restrict__ st2,
                                              const float* __restrict__ mod, u16* __restrict__ hh) {
  extern __shared__ __attribute__((aligned(16))) char lds[];
  const int b = blockIdx.x, xcd = b & 7, idx = b >> 3;
  const int bm = idx & 7, bn = xcd * 4 + (idx >> 3);
  const int n0 = bn * 144;
  EpiGLU epi{hh, n0};
  gemm_body<256, 288, 2, 8, 1, 32, 4, 2>(lds, x1, D_, bm * 256, 0, D_ / 32, st2, mod + 4 * D_, mod + 3 * D_, wg + n0, wu + n0, F_, epi);
}

struct EpiPart {
  float* part; int n0;
  template <int NT>
  DI void run(f32x4 (&acc)[2][NT], int row0, int fq) const {
#pragma unroll
    for (int m = 0; m < 2; ++m)
#pragma unroll
      for (int n = 0; n < NT; ++n)
        *reinterpret_cast<f32x4*>(part + (size_t)(row0 + m * 16) * D_ + n0 + n * 16 + fq * 4) = acc[m][n];
  }
};

constexpr int LDS_DOWN = 4 * (128 * 64 * 2 + 64 * 144 * 2);
__global__ __launch_bounds__(512) void k_down(const u16* __restrict__ hh, const u16* __restrict__ wd,
                                               float* __restrict__ part) {
  extern __shared__ __attribute__((aligned(16))) char lds[];
  const int b = blockIdx.x, xcd = b & 7, idx = b >> 3;
  const int sk = xcd >> 1, bn = idx & 3, bm = (xcd & 1) * 8 + (idx >> 2);
  const int n0 = bn * 144;
  EpiPart epi{part + (size_t)sk * S_ * D_, n0};
  gemm_body<128, 144, 1, 4, 2, 64, 4, 0>(lds, hh, F_, bm * 128, sk * (F_ / 4), F_ / 4 / 64, nullptr, nullptr, nullptr, wd + n0, wd + n0, D_, epi);
}

__global__ __launch_bounds__(256) void k_final(const float* __restrict__ part, const u16* __restrict__ x1,
                                                const float* __restrict__ mod, u16* __restrict__ out) {
  const int i = (blockIdx.x * 256 + threadIdx.x) * 8;
  if (i >= S_ * D_) return;
  const int col = i % D_;
  const float* gb = mod + 5 * D_ + col;
  u32x4 xv = *reinterpret_cast<const u32x4*>(x1 + i);
  u32x4 o;
#pragma unroll
  for (int j = 0; j < 4; ++j) {
    float a = 0.f, b = 0.f;
#pragma unroll
    for (int s = 0; s < 4; ++s) {
      f32x2 p = *reinterpret_cast<const f32x2*>(part + (size_t)s * S_ * D_ + i + 2 * j);
      a += p[0]; b += p[1];
    }
    o[j] = pack2(bflo(xv[j]) + gb[2 * j] * a, bfhi(xv[j]) + gb[2 * j + 1] * b);
  }
  *reinterpret_cast<u32x4*>(out + i) = o;
}

extern "C" void kernel_launch(void* const* d_in, const int* in_sizes, int n_in,
                              void* d_out, int out_size, void* d_ws, size_t ws_size,
                              hipStream_t stream) {
  const u16* x = (const u16*)d_in[0];
  const u16* wqkv = (const u16*)d_in[1];
  const u16* wo = (const u16*)d_in[2];
  const u16* wg = (const u16*)d_in[4];
  const u16* wu = (const u16*)d_in[5];
  const u16* wd = (const u16*)d_in[6];
  const u16* c = (const u16*)d_in[7];
  const u16* wmod = (const u16*)d_in[8];
  char* ws = (char*)d_ws;
  float* mod = (float*)(ws + WS_MOD);
  float2* st1 = (float2*)(ws + WS_ST1);
  float2* st2 = (float2*)(ws + WS_ST2);
  u16* Q = (u16*)(ws + WS_Q);
  u16* K = (u16*)(ws + WS_K);
  u16* V = (u16*)(ws + WS_V);
  u16* O = (u16*)(ws + WS_O);
  u16* x1 = (u16*)(ws + WS_X1);
  u16* hh = (u16*)(ws + WS_HH);
  float* part = (float*)(ws + WS_PART);

  hipFuncSetAttribute((const void*)k_qkv, hipFuncAttributeMaxDynamicSharedMemorySize, LDS_QKV);
  hipFuncSetAttribute((const void*)k_proj, hipFuncAttributeMaxDynamicSharedMemorySize, LDS_PROJ);
  hipFuncSetAttribute((const void*)k_glu, hipFuncAttributeMaxDynamicSharedMemorySize, LDS_GLU);
  hipFuncSetAttribute((const void*)k_down, hipFuncAttributeMaxDynamicSharedMemorySize, LDS_DOWN);

  k_prep<<<108 + 512, 256, 0, stream>>>(c, wmod, x, mod, st1);
  k_qkv<<<192, 512, LDS_QKV, stream>>>(x, wqkv, st1, mod, Q);
  k_attn<<<32 * 16, 256, 0, stream>>>(Q, K, V, O);
  k_proj<<<256, 128, LDS_PROJ, stream>>>(O, wo, x, mod, x1, st2);
  k_glu<<<256, 512, LDS_GLU, stream>>>(x1, wg, wu, st2, mod, hh);
  k_down<<<256, 512, LDS_DOWN, stream>>>(hh, wd, part);
  k_final<<<(S_ * D_ / 8 + 255) / 256, 256, 0, stream>>>(part, x1, mod, (u16*)d_out);
}
```

```cpp
#include <hip/hip_runtime.h>
#include <hip/hip_bf16.h>
#include <stdint.h>
#include <type_traits>


typedef __attribute__((ext_vector_type(8))) short bf16x8;
typedef __attribute__((ext_vector_type(4))) short s16x4;
typedef __attribute__((ext_vector_type(4))) float f32x4;
typedef __attribute__((ext_vector_type(2))) float f32x2;
typedef __attribute__((ext_vector_type(16))) float f32x16;
typedef __attribute__((ext_vector_type(4))) unsigned u32x4;
typedef __attribute__((ext_vector_type(2))) unsigned u32x2;
typedef __attribute__((ext_vector_type(2))) __bf16 bf16x2_t;
typedef unsigned short u16;

#define DI __device__ __forceinline__

constexpr int S_ = 2048, D_ = 576, H_ = 32, DH_ = 18, F_ = 4608;
constexpr int HP = 32;
constexpr float QSCALE = 0.23570226039551584f * 1.4426950408889634f;

DI float bf2f(u16 v) { return __uint_as_float(((unsigned)v) << 16); }
DI float bflo(unsigned v) { return __uint_as_float(v << 16); }
DI float bfhi(unsigned v) { return __uint_as_float(v & 0xffff0000u); }
DI unsigned pack2(float a, float b) {
  f32x2 v = {a, b};
  bf16x2_t r = __builtin_convertvector(v, bf16x2_t);
  return __builtin_bit_cast(unsigned, r);
}
DI u16 f2bf(float a) { return (u16)(pack2(a, 0.f) & 0xffffu); }
DI float rbf(float a) { return bflo(pack2(a, 0.f) & 0xffffu); }

typedef __attribute__((address_space(3))) s16x4* lds_s16x4_ptr;
DI s16x4 tr_read(const char* p) {
  return __builtin_amdgcn_ds_read_tr16_b64_v4i16((lds_s16x4_ptr)(p));
}

constexpr size_t WS_MOD = 0;
constexpr size_t WS_ST1 = 16384;
constexpr size_t WS_ST2 = WS_ST1 + 2048 * 8;
constexpr size_t WS_Q = 1 << 20;
constexpr size_t WS_K = WS_Q + (size_t)H_ * S_ * HP * 2;
constexpr size_t WS_V = WS_K + (size_t)H_ * S_ * HP * 2;
constexpr size_t WS_O = WS_V + (size_t)H_ * S_ * HP * 2;
constexpr size_t WS_X1 = WS_O + (size_t)S_ * D_ * 2;
constexpr size_t WS_H2 = WS_X1 + (size_t)S_ * D_ * 2;
constexpr size_t WS_HH = WS_H2 + (size_t)S_ * D_ * 2;
constexpr size_t WS_PART = WS_HH + (size_t)S_ * F_ * 2;

__global__ __launch_bounds__(256) void k_prep(const u16* __restrict__ c, const u16* __restrict__ wmod,
                                               const u16* __restrict__ x, float* __restrict__ mod,
                                               float2* __restrict__ st1) {
  __shared__ float red[64][33];
  const int t = threadIdx.x;
  if (blockIdx.x < 108) {
    const int n0 = blockIdx.x * 32 + (t & 3) * 8;
    const int ks = t >> 2;
    float acc[8];
#pragma unroll
    for (int j = 0; j < 8; ++j) acc[j] = 0.f;
#pragma unroll
    for (int r = 0; r < 9; ++r) {
      const int k = ks * 9 + r;
      float cv = bf2f(c[k]);
      float sv = rbf(cv / (1.f + __expf(-cv)));
      u32x4 w = *reinterpret_cast<const u32x4*>(wmod + (size_t)k * 3456 + n0);
#pragma unroll
      for (int j = 0; j < 4; ++j) {
        acc[2 * j] += sv * bflo(w[j]);
        acc[2 * j + 1] += sv * bfhi(w[j]);
      }
    }
#pragma unroll
    for (int j = 0; j < 8; ++j) red[ks][(t & 3) * 8 + j] = acc[j];
    __syncthreads();
    if (t < 32) {
      float s = 0.f;
#pragma unroll 8
      for (int i = 0; i < 64; ++i) s += red[i][t];
      mod[blockIdx.x * 32 + t] = rbf(s);
    }
  } else {
    const int lane = t & 63, w = t >> 6;
    for (int row = (blockIdx.x - 108) * 4 + w; row < S_; row += (gridDim.x - 108) * 4) {
      const u16* xr = x + (size_t)row * D_;
      u32x4 a = *reinterpret_cast<const u32x4*>(xr + lane * 8);
      u32x4 b = {0, 0, 0, 0};
      if (lane < 8) b = *reinterpret_cast<const u32x4*>(xr + 512 + lane * 8);
      float v[16];
#pragma unroll
      for (int j = 0; j < 4; ++j) {
        v[2 * j] = bflo(a[j]); v[2 * j + 1] = bfhi(a[j]);
        v[8 + 2 * j] = bflo(b[j]); v[8 + 2 * j + 1] = bfhi(b[j]);
      }
      float s = 0.f;
#pragma unroll
      for (int j = 0; j < 16; ++j) s += v[j];
#pragma unroll
      for (int o = 32; o >= 1; o >>= 1) s += __shfl_xor(s, o);
      const float mean = s * (1.f / D_);
      float q = 0.f;
#pragma unroll
      for (int j = 0; j < 8; ++j) { float d = v[j] - mean; q += d * d; }
      if (lane < 8) {
#pragma unroll
        for (int j = 8; j < 16; ++j) { float d = v[j] - mean; q += d * d; }
      }
#pragma unroll
      for (int o = 32; o >= 1; o >>= 1) q += __shfl_xor(q, o);
      if (lane == 0) st1[row] = make_float2(mean, rsqrtf(q * (1.f / D_) + 1e-5f));
    }
  }
}

#define WAIT_LGKM0() asm volatile("s_waitcnt lgkmcnt(0)" ::: "memory")
#define RAW_BAR() __builtin_amdgcn_s_barrier()
template <int N> DI void wait_vm() { asm volatile("s_waitcnt vmcnt(%0)" ::"n"(N) : "memory"); }
template <int OFF> DI void lds_tr(u32x2& d, unsigned a) { asm volatile("ds_read_b64_tr_b16 %0, %1 offset:%2" : "=v"(d) : "v"(a), "n"(OFF)); }
template <int OFF> DI void lds_128(u32x4& d, unsigned a) { asm volatile("ds_read_b128 %0, %1 offset:%2" : "=v"(d) : "v"(a), "n"(OFF)); }
template <int N> DI void wait_l2(u32x2& a, u32x2& b) { asm volatile("s_waitcnt lgkmcnt(%2)" : "+v"(a), "+v"(b) : "n"(N)); }
template <int N> DI void wait_l4(u32x2& a, u32x2& b, u32x4& c, u32x4& d) { asm volatile("s_waitcnt lgkmcnt(%4)" : "+v"(a), "+v"(b), "+v"(c), "+v"(d) : "n"(N)); }

template <int BK> DI int a_swz(int row) { return BK == 64 ? ((row >> 1) & 7) : ((-(row >> 2)) & 3); }

template <int I, int N, class F> DI void static_for(F&& f) {
  if constexpr (I < N) { f(std::integral_constant<int, I>{}); static_for<I + 1, N>(f); }
}

template <int BM, int BN, int NIMG, int WM, int WK, int BK, int NST, int LNMODE  , class Epi>
DI void gemm_body(char* lds, const u16* __restrict__ A, int lda, int m0, int kbeg, int nk,
                  const float2* __restrict__ stats, const float* __restrict__ mscale, const float* __restrict__ mshift,
                  const u16* __restrict__ B0, const u16* __restrict__ B1, int ldb, Epi& epi) {
  constexpr int NTHR = 64 * WM * WK, NW = WM * WK;
  constexpr int NT = BN / 16, BNI = BN / NIMG, NTI = NT / NIMG;
  constexpr int ABYTES = BM * BK * 2, BBYTES = BK * BN * 2, STAGE = ABYTES + BBYTES;
  constexpr int AINS = ABYTES / 1024, BINS = BBYTES / 1024;
  constexpr int BINS_I = BINS / NIMG;
  constexpr int TINS = AINS + BINS;
  constexpr int G = (TINS + NW - 1) / NW;
  constexpr int SUB = BK / 32 / WK;
  constexpr int CPR = BK / 8;
  static_assert(BBYTES % (1024 * NIMG) == 0 && ABYTES % 1024 == 0 && (BK == 32 || BK == 64), "tile");
  const int t = threadIdx.x, lane = t & 63;
  const int wid = __builtin_amdgcn_readfirstlane(t >> 6);
  const int wr = wid % WM, wk = wid / WM, fr = lane & 15, fq = lane >> 4;
  float* lnp = reinterpret_cast<float*>(lds + NST * STAGE);

  unsigned goff[G];
#pragma unroll
  for (int ii = 0; ii < G; ++ii) {
    int i = wid + ii * NW;
    if (i >= TINS) i -= TINS;
    if (i < AINS) {
      const int j = i * 64 + lane;
      const int row = j / CPR, pos = j % CPR;
      goff[ii] = (unsigned)((row * lda + (pos ^ a_swz<BK>(row)) * 8) * 2);
    } else {
      const int j = ((i - AINS) % BINS_I) * 64 + lane;
      const int kb = j / BNI, rem = j % BNI;
      const int sub = rem >> 4, r = (rem >> 1) & 7, hf = rem & 1;
      goff[ii] = (unsigned)(((kb * 8 + (r ^ ((kb & 1) << 2))) * ldb + sub * 16 + hf * 8) * 2);
    }
  }
  const char* Abase = (const char*)(A + (size_t)m0 * lda + kbeg);
  const char* B0base = (const char*)(B0 + (size_t)kbeg * ldb);
  const char* B1base = (const char*)(B1 + (size_t)kbeg * ldb);

  auto stage_issue = [&](int kt) {
    char* buf = lds + (kt % NST) * STAGE;
    const size_t bk = (size_t)kt * BK * ldb * 2;
    const char* ab = Abase + kt * BK * 2;
#pragma unroll
    for (int ii = 0; ii < G; ++ii) {
      int i = wid + ii * NW;
      if (i >= TINS) i -= TINS;
      const char* base = (i < AINS) ? ab : (((NIMG == 2 && i - AINS >= BINS_I) ? B1base : B0base) + bk);
      __builtin_amdgcn_global_load_lds((const unsigned*)(base + goff[ii]),
                                       (__attribute__((address_space(3))) unsigned*)(buf + (i * 64 + lane) * 16), 16, 0, 0);
    }
  };

#pragma unroll
  for (int st = 0; st < NST - 1; ++st)
    if (st < nk) stage_issue(st);

  float ra[2], rb[2];
  if constexpr (LNMODE != 0) {
    for (int i = t; i < D_; i += NTHR) { lnp[i] = mscale[i] + 1.f; lnp[D_ + i] = mshift[i]; }
#pragma unroll
    for (int m = 0; m < 2; ++m) {
      float2 s = stats[m0 + wr * 32 + m * 16 + fr];
      ra[m] = s.y; rb[m] = -s.x * s.y;
    }
    wait_vm<0>();
    WAIT_LGKM0();
  }

  f32x4 acc[2][NT];
#pragma unroll
  for (int m = 0; m < 2; ++m)
#pragma unroll
    for (int n = 0; n < NT; ++n) acc[m][n] = f32x4{0.f, 0.f, 0.f, 0.f};

  const unsigned lbase = (unsigned)(size_t)lds;
  const int q4 = fr >> 2, p4 = lane & 3, sw = (fq & 1) << 2;
  const int arow0 = wr * 32 + fr, arow1 = arow0 + 16;
  const unsigned aoff0 = arow0 * (BK * 2), aoff1 = arow1 * (BK * 2);
  const int asw0 = a_swz<BK>(arow0), asw1 = a_swz<BK>(arow1);
  const unsigned boff_lo = ABYTES + (wk * 4 + fq) * (BNI * 16) + ((q4 ^ sw) << 5) + p4 * 8;
  const unsigned boff_hi = ABYTES + (wk * 4 + fq) * (BNI * 16) + (((q4 + 4) ^ sw) << 5) + p4 * 8;

  constexpr int PF = 4;
  constexpr int T = SUB * NT;
  for (int kt = 0; kt < nk; ++kt) {
    if (kt + NST - 2 < nk) wait_vm<G * (NST - 2)>(); else wait_vm<0>();
    RAW_BAR();
    if (kt + NST - 1 < nk) stage_issue(kt + NST - 1);
    const unsigned sb = lbase + (kt % NST) * STAGE;
    const unsigned vb_lo = sb + boff_lo, vb_hi = sb + boff_hi;
    u32x2 blo[PF], bhi[PF];
    u32x4 af[2][2];
    auto rda = [&](auto S2) {
      constexpr int s2 = decltype(S2)::value;
      const int ch = (wk + s2 * WK) * 4 + fq;
      lds_128<0>(af[s2 & 1][0], sb + aoff0 + ((ch ^ asw0) << 4));
      lds_128<0>(af[s2 & 1][1], sb + aoff1 + ((ch ^ asw1) << 4));
    };
    auto rdb = [&](auto IDX) {
      constexpr int idx = decltype(IDX)::value;
      constexpr int s2 = idx / NT, n = idx % NT;
      constexpr int off = s2 * WK * 4 * (BNI * 16) + (n / NTI) * (BBYTES / NIMG) + (n % NTI) * 256;
      lds_tr<off>(blo[idx % PF], vb_lo);
      lds_tr<off>(bhi[idx % PF], vb_hi);
    };
    rda(std::integral_constant<int, 0>{});
    static_for<0, PF - 1>([&](auto I) { rdb(I); });
    static_for<0, T>([&](auto IDX) {
      constexpr int idx = decltype(IDX)::value;
      constexpr int s2 = idx / NT, n = idx % NT;
      if constexpr (n == NT - PF && s2 + 1 < SUB) rda(std::integral_constant<int, s2 + 1>{});
      if constexpr (idx + PF - 1 < T) rdb(std::integral_constant<int, idx + PF - 1>{});
      constexpr int nb = ((idx + PF - 1 < T) ? (idx + PF - 1) : (T - 1)) - idx;
      constexpr int sA = (idx + PF) / NT - 1;
      constexpr int idxA = sA * NT + NT - PF;
      constexpr bool a_after = (sA >= 0) && (sA + 1 < SUB) && (idxA <= idx) && (idx <= idxA + PF - 2);
      constexpr int cnt = 2 * nb + (a_after ? 2 : 0);
      if constexpr (n == 0) {
        wait_l4<cnt>(blo[idx % PF], bhi[idx % PF], af[s2 & 1][0], af[s2 & 1][1]);
        if constexpr (LNMODE != 0) {
          const int kc = kbeg + kt * BK + (wk + s2 * WK) * 32 + fq * 8;
          f32x4 sc0 = *reinterpret_cast<const f32x4*>(lnp + kc), sc1 = *reinterpret_cast<const f32x4*>(lnp + kc + 4);
          f32x4 sh0 = *reinterpret_cast<const f32x4*>(lnp + D_ + kc), sh1 = *reinterpret_cast<const f32x4*>(lnp + D_ + kc + 4);
#pragma unroll
          for (int m = 0; m < 2; ++m) {
            u32x4 r = af[s2 & 1][m], o;
            const float a = ra[m], b = rb[m];
            o[0] = pack2((bflo(r[0]) * a + b) * sc0[0] + sh0[0], (bfhi(r[0]) * a + b) * sc0[1] + sh0[1]);
            o[1] = pack2((bflo(r[1]) * a + b) * sc0[2] + sh0[2], (bfhi(r[1]) * a + b) * sc0[3] + sh0[3]);
            o[2] = pack2((bflo(r[2]) * a + b) * sc1[0] + sh1[0], (bfhi(r[2]) * a + b) * sc1[1] + sh1[1]);
            o[3] = pack2((bflo(r[3]) * a + b) * sc1[2] + sh1[2], (bfhi(r[3]) * a + b) * sc1[3] + sh1[3]);
            af[s2 & 1][m] = o;
          }
        }
      } else {
        wait_l2<cnt>(blo[idx % PF], bhi[idx % PF]);
      }
      u32x4 bw = {blo[idx % PF][0], blo[idx % PF][1], bhi[idx % PF][0], bhi[idx % PF][1]};
      const bf16x8 bf = __builtin_bit_cast(bf16x8, bw);
      acc[0][n] = __builtin_amdgcn_mfma_f32_16x16x32_bf16(bf, __builtin_bit_cast(bf16x8, af[s2 & 1][0]), acc[0][n], 0, 0, 0);
      acc[1][n] = __builtin_amdgcn_mfma_f32_16x16x32_bf16(bf, __builtin_bit_cast(bf16x8, af[s2 & 1][1]), acc[1][n], 0, 0, 0);
    });
  }
  __syncthreads();
  if constexpr (WK > 1) {
    f32x4* red = reinterpret_cast<f32x4*>(lds);
    if (wk > 0) {
#pragma unroll
      for (int m = 0; m < 2; ++m)
#pragma unroll
        for (int n = 0; n < NT; ++n) red[(((wk - 1) * WM + wr) * 2 * NT + m * NT + n) * 64 + lane] = acc[m][n];
    }
    __syncthreads();
    if (wk == 0) {
#pragma unroll
      for (int g = 1; g < WK; ++g)
#pragma unroll
        for (int m = 0; m < 2; ++m)
#pragma unroll
          for (int n = 0; n < NT; ++n) acc[m][n] += red[(((g - 1) * WM + wr) * 2 * NT + m * NT + n) * 64 + lane];
    }
    __syncthreads();
  }
  epi.template run<NT, NTHR>(acc, lds, wk == 0, wr * 32 + fr, fq);
}

struct EpiQKV {
  u16* qkv;
  int m0, n0;
  template <int NT, int NTHR>
  DI void run(f32x4 (&acc)[2][NT], char* lds, bool active, int row0, int fq) const {
    constexpr int RS = 296;
    const int which = n0 / D_;
    const float sc = which == 0 ? QSCALE : 1.f;
    if (active) {
#pragma unroll
      for (int m = 0; m < 2; ++m)
#pragma unroll
        for (int n = 0; n < NT; ++n) {
          u32x2 o;
          o[0] = pack2(acc[m][n][0] * sc, acc[m][n][1] * sc);
          o[1] = pack2(acc[m][n][2] * sc, acc[m][n][3] * sc);
          *reinterpret_cast<u32x2*>(lds + (row0 + m * 16) * RS + (n * 16 + fq * 4) * 2) = o;
        }
    }
    __syncthreads();
    const int head0 = (n0 % D_) / DH_;
    const unsigned padw = (which == 2) ? 0x00003f80u : 0u;
    for (int it = threadIdx.x; it < 128 * 8; it += NTHR) {
      const int row = it >> 3, hd = it & 7;
      const unsigned* src = reinterpret_cast<const unsigned*>(lds + row * RS + hd * 36);
      u32x4 w0 = {src[0], src[1], src[2], src[3]};
      u32x4 w1 = {src[4], src[5], src[6], src[7]};
      u32x4 w2 = {src[8], padw, 0u, 0u};
      u32x4 w3 = {0u, 0u, 0u, 0u};
      u32x4* dst = reinterpret_cast<u32x4*>(qkv + ((size_t)(which * H_ + head0 + hd) * S_ + m0 + row) * HP);
      dst[0] = w0; dst[1] = w1; dst[2] = w2; dst[3] = w3;
    }
  }
};

constexpr int LDS_QKV = 4 * (128 * 64 * 2 + 64 * 144 * 2) + 2 * D_ * 4;
__global__ __launch_bounds__(512) void k_qkv(const u16* __restrict__ x, const u16* __restrict__ wqkv,
                                              const float2* __restrict__ st1, const float* __restrict__ mod,
                                              u16* __restrict__ qkv) {
  extern __shared__ __attribute__((aligned(16))) char lds[];
  const int b = blockIdx.x;
  const int xcd = b & 7, idx = b >> 3;
  const int bn = (idx % 3) * 4 + (xcd >> 1), bm = (idx / 3) * 2 + (xcd & 1);
  const int n0 = bn * 144;
  EpiQKV epi{qkv, bm * 128, n0};
  gemm_body<128, 144, 1, 4, 2, 64, 4, 1>(lds, x, D_, bm * 128, 0, D_ / 64, st1, mod + D_, mod, wqkv + n0, wqkv + n0, 3 * D_, epi);
}

__global__ __launch_bounds__(256) void k_attn(const u16* __restrict__ Q, const u16* __restrict__ K,
                                               const u16* __restrict__ V, u16* __restrict__ O) {
  __shared__ __attribute__((aligned(16))) char lds[2 * 8192];
  const int t = threadIdx.x, lane = t & 63, wid = t >> 6, r32 = lane & 31, h = lane >> 5;
  const int head = blockIdx.x >> 4, qb = blockIdx.x & 15;
  const int q0 = qb * 128 + wid * 32;
  const u16* Qh = Q + (size_t)head * S_ * HP;
  const u16* Kh = K + (size_t)head * S_ * HP;
  const u16* Vh = V + (size_t)head * S_ * HP;
  bf16x8 qf0 = *reinterpret_cast<const bf16x8*>(Qh + (size_t)(q0 + r32) * HP + 8 * h);
  bf16x8 qf1 = *reinterpret_cast<const bf16x8*>(Qh + (size_t)(q0 + r32) * HP + 16 + 8 * h);

  const int skey = t >> 2, sch = t & 3;
  const int kst_off = skey * 64 + ((sch ^ ((skey >> 2) & 3)) << 4);
  const int vst_off = 4096 + skey * 64 + sch * 16;
  u32x4 kreg, vreg;
  auto gload = [&](int kt) {
    kreg = *reinterpret_cast<const u32x4*>(Kh + (size_t)(kt * 64 + skey) * HP + sch * 8);
    vreg = *reinterpret_cast<const u32x4*>(Vh + (size_t)(kt * 64 + skey) * HP + sch * 8);
  };
  auto swrite = [&](char* buf) {
    *reinterpret_cast<u32x4*>(buf + kst_off) = kreg;
    *reinterpret_cast<u32x4*>(buf + vst_off) = vreg;
  };

  f32x16 oacc;
#pragma unroll
  for (int i = 0; i < 16; ++i) oacc[i] = 0.f;
  float m_run = -1e30f;
  constexpr float THR = 8.f;

  const int kr0 = r32 * 64 + (((0 + h) ^ ((r32 >> 2) & 3)) << 4);
  const int kr1 = r32 * 64 + (((2 + h) ^ ((r32 >> 2) & 3)) << 4);
  const int g = lane >> 4, q4 = (lane & 15) >> 2, p4 = lane & 3;
  const int vr = 4096 + (4 * (g >> 1) + q4) * 64 + (g & 1) * 32 + p4 * 8;

  gload(0);
  swrite(lds);
  __syncthreads();
  constexpr int NT = S_ / 64;
  for (int kt = 0; kt < NT; ++kt) {
    char* cur = lds + (kt & 1) * 8192;
    char* nxt = lds + ((kt + 1) & 1) * 8192;
    if (kt + 1 < NT) gload(kt + 1);
    f32x16 s0, s1;
#pragma unroll
    for (int i = 0; i < 16; ++i) { s0[i] = 0.f; s1[i] = 0.f; }
    {
      bf16x8 ka = *reinterpret_cast<const bf16x8*>(cur + kr0);
      bf16x8 kb = *reinterpret_cast<const bf16x8*>(cur + kr1);
      bf16x8 kc = *reinterpret_cast<const bf16x8*>(cur + 2048 + kr0);
      bf16x8 kd = *reinterpret_cast<const bf16x8*>(cur + 2048 + kr1);
      s0 = __builtin_amdgcn_mfma_f32_32x32x16_bf16(ka, qf0, s0, 0, 0, 0);
      s0 = __builtin_amdgcn_mfma_f32_32x32x16_bf16(kb, qf1, s0, 0, 0, 0);
      s1 = __builtin_amdgcn_mfma_f32_32x32x16_bf16(kc, qf0, s1, 0, 0, 0);
      s1 = __builtin_amdgcn_mfma_f32_32x32x16_bf16(kd, qf1, s1, 0, 0, 0);
    }
    float mt = fmaxf(s0[0], s1[0]);
#pragma unroll
    for (int i = 1; i < 16; ++i) mt = fmaxf(mt, fmaxf(s0[i], s1[i]));
    mt = fmaxf(mt, __shfl_xor(mt, 32));
    if (!__all(mt - m_run <= THR)) {
      const float mn = fmaxf(m_run, mt);
      const float alpha = __builtin_amdgcn_exp2f(m_run - mn);
      m_run = mn;
#pragma unroll
      for (int i = 0; i < 16; ++i) oacc[i] *= alpha;
    }
    u32x4 pf[4];
#pragma unroll
    for (int i = 0; i < 8; ++i) {
      float a = __builtin_amdgcn_exp2f(s0[2 * i] - m_run), b = __builtin_amdgcn_exp2f(s0[2 * i + 1] - m_run);
      float c = __builtin_amdgcn_exp2f(s1[2 * i] - m_run), d = __builtin_amdgcn_exp2f(s1[2 * i + 1] - m_run);
      pf[i >> 2][i & 3] = pack2(a, b);
      pf[2 + (i >> 2)][i & 3] = pack2(c, d);
    }
#pragma unroll
    for (int u = 0; u < 4; ++u) {
      s16x4 lo = tr_read(cur + vr + (16 * u) * 64);
      s16x4 hi = tr_read(cur + vr + (16 * u + 8) * 64);
      bf16x8 vf = __builtin_shufflevector(lo, hi, 0, 1, 2, 3, 4, 5, 6, 7);
      oacc = __builtin_amdgcn_mfma_f32_32x32x16_bf16(vf, __builtin_bit_cast(bf16x8, pf[u]), oacc, 0, 0, 0);
    }
    if (kt + 1 < NT) swrite(nxt);
    __syncthreads();
  }
  float l = __shfl(oacc[10], r32);
  const float inv = 1.f / l;
  unsigned* op = reinterpret_cast<unsigned*>(O + (size_t)(q0 + r32) * D_ + head * DH_);
  if (h == 0) {
    op[0] = pack2(oacc[0] * inv, oacc[1] * inv);
    op[1] = pack2(oacc[2] * inv, oacc[3] * inv);
    op[4] = pack2(oacc[4] * inv, oacc[5] * inv);
    op[5] = pack2(oacc[6] * inv, oacc[7] * inv);
    op[8] = pack2(oacc[8] * inv, oacc[9] * inv);
  } else {
    op[2] = pack2(oacc[0] * inv, oacc[1] * inv);
    op[3] = pack2(oacc[2] * inv, oacc[3] * inv);
    op[6] = pack2(oacc[4] * inv, oacc[5] * inv);
    op[7] = pack2(oacc[6] * inv, oacc[7] * inv);
  }
}

struct EpiProj {
  const u16* x; const float* ga; u16* x1; float2* st2; int m0, n0;
  template <int NT, int NTHR>
  DI void run(f32x4 (&acc)[2][NT], char* lds, bool active, int row0, int fq) const {
    if (!active) return;
#pragma unroll
    for (int m = 0; m < 2; ++m) {
      const int row = m0 + row0 + m * 16;
      float v[NT][4];
      float s = 0.f;
#pragma unroll
      for (int n = 0; n < NT; ++n) {
        const int col = n0 + n * 16 + fq * 4;
        u32x2 xv = *reinterpret_cast<const u32x2*>(x + (size_t)row * D_ + col);
        f32x4 g = *reinterpret_cast<const f32x4*>(ga + col);
        u32x2 o;
        o[0] = pack2(bflo(xv[0]) + g[0] * acc[m][n][0], bfhi(xv[0]) + g[1] * acc[m][n][1]);
        o[1] = pack2(bflo(xv[1]) + g[2] * acc[m][n][2], bfhi(xv[1]) + g[3] * acc[m][n][3]);
        *reinterpret_cast<u32x2*>(x1 + (size_t)row * D_ + col) = o;
        v[n][0] = bflo(o[0]); v[n][1] = bfhi(o[0]); v[n][2] = bflo(o[1]); v[n][3] = bfhi(o[1]);
        s += (v[n][0] + v[n][1]) + (v[n][2] + v[n][3]);
      }
      s += __shfl_xor(s, 16); s += __shfl_xor(s, 32);
      const float lm = s * (1.f / (16 * NT));
      float q = 0.f;
#pragma unroll
      for (int n = 0; n < NT; ++n)
#pragma unroll
        for (int j = 0; j < 4; ++j) { float d = v[n][j] - lm; q += d * d; }
      q += __shfl_xor(q, 16); q += __shfl_xor(q, 32);
      if (fq == 0) st2[(size_t)row * 4 + n0 / 144] = make_float2(s, q);
    }
  }
};

constexpr int LDS_PROJ = 4 * (32 * 64 * 2 + 64 * 144 * 2);
__global__ __launch_bounds__(128) void k_proj(const u16* __restrict__ o, const u16* __restrict__ wo,
                                               const u16* __restrict__ x, const float* __restrict__ mod,
                                               u16* __restrict__ x1, float2* __restrict__ st2) {
  extern __shared__ __attribute__((aligned(16))) char lds[];
  const int b = blockIdx.x;
  const int bn = b & 3, bm = b >> 2;
  const int n0 = bn * 144;
  EpiProj epi{x, mod + 2 * D_, x1, st2, bm * 32, n0};
  gemm_body<32, 144, 1, 1, 2, 64, 4, 0>(lds, o, D_, bm * 32, 0, D_ / 64, nullptr, nullptr, nullptr, wo + n0, wo + n0, D_, epi);
}

__global__ __launch_bounds__(256) void k_ln2(const u16* __restrict__ x1, const float2* __restrict__ st2,
                                              const float* __restrict__ mod, u16* __restrict__ h2) {
  const int row = blockIdx.x * 4 + (threadIdx.x >> 6), lane = threadIdx.x & 63;
  const float2* p = st2 + (size_t)row * 4;
  float2 p0 = p[0], p1 = p[1], p2 = p[2], p3 = p[3];
  const float mean = (p0.x + p1.x + p2.x + p3.x) * (1.f / D_);
  float d0 = p0.x * (1.f / 144.f) - mean, d1 = p1.x * (1.f / 144.f) - mean, d2 = p2.x * (1.f / 144.f) - mean, d3 = p3.x * (1.f / 144.f) - mean;
  float m2 = p0.y + p1.y + p2.y + p3.y + 144.f * (d0 * d0 + d1 * d1 + d2 * d2 + d3 * d3);
  const float a = rsqrtf(m2 * (1.f / D_) + 1e-5f), b = -mean * a;
  const float* sb = mod + 3 * D_;
  const float* cb = mod + 4 * D_;
  for (int c = lane; c < 72; c += 64) {
    u32x4 r = *reinterpret_cast<const u32x4*>(x1 + (size_t)row * D_ + c * 8);
    f32x4 sc0 = *reinterpret_cast<const f32x4*>(cb + c * 8), sc1 = *reinterpret_cast<const f32x4*>(cb + c * 8 + 4);
    f32x4 sh0 = *reinterpret_cast<const f32x4*>(sb + c * 8), sh1 = *reinterpret_cast<const f32x4*>(sb + c * 8 + 4);
    u32x4 o;
    o[0] = pack2((bflo(r[0]) * a + b) * (sc0[0] + 1.f) + sh0[0], (bfhi(r[0]) * a + b) * (sc0[1] + 1.f) + sh0[1]);
    o[1] = pack2((bflo(r[1]) * a + b) * (sc0[2] + 1.f) + sh0[2], (bfhi(r[1]) * a + b) * (sc0[3] + 1.f) + sh0[3]);
    o[2] = pack2((bflo(r[2]) * a + b) * (sc1[0] + 1.f) + sh1[0], (bfhi(r[2]) * a + b) * (sc1[1] + 1.f) + sh1[1]);
    o[3] = pack2((bflo(r[3]) * a + b) * (sc1[2] + 1.f) + sh1[2], (bfhi(r[3]) * a + b) * (sc1[3] + 1.f) + sh1[3]);
    *reinterpret_cast<u32x4*>(h2 + (size_t)row * D_ + c * 8) = o;
  }
}

struct EpiGLU {
  u16* hh; int m0, n0;
  template <int NT, int NTHR>
  DI void run(f32x4 (&acc)[2][NT], char* lds, bool active, int row0, int fq) const {
    constexpr int RS = 296;
#pragma unroll
    for (int m = 0; m < 2; ++m)
#pragma unroll
      for (int n = 0; n < NT / 2; ++n) {
        u32x2 o;
        o[0] = pack2(acc[m][n][0] * acc[m][n + NT / 2][0], acc[m][n][1] * acc[m][n + NT / 2][1]);
        o[1] = pack2(acc[m][n][2] * acc[m][n + NT / 2][2], acc[m][n][3] * acc[m][n + NT / 2][3]);
        *reinterpret_cast<u32x2*>(lds + (row0 + m * 16) * RS + (n * 16 + fq * 4) * 2) = o;
      }
    __syncthreads();
    for (int it = threadIdx.x; it < 256 * 18; it += NTHR) {
      const int row = it / 18, c = it % 18;
      const u32x2* src = reinterpret_cast<const u32x2*>(lds + row * RS + c * 16);
      u32x2 lo = src[0], hi = src[1];
      u32x4 w = {lo[0], lo[1], hi[0], hi[1]};
      *reinterpret_cast<u32x4*>(hh + (size_t)(m0 + row) * F_ + n0 + c * 8) = w;
    }
  }
};

constexpr int LDS_GLU = 4 * (256 * 32 * 2 + 32 * 288 * 2);
__global__ __launch_bounds__(512) void k_glu(const u16* __restrict__ h2, const u16* __restrict__ wg,
                                              const u16* __restrict__ wu, u16* __restrict__ hh) {
  extern __shared__ __attribute__((aligned(16))) char lds[];
  const int b = blockIdx.x, xcd = b & 7, idx = b >> 3;
  const int bm = idx & 7, bn = xcd * 4 + (idx >> 3);
  const int n0 = bn * 144;
  EpiGLU epi{hh, bm * 256, n0};
  gemm_body<256, 288, 2, 8, 1, 32, 4, 0>(lds, h2, D_, bm * 256, 0, D_ / 32, nullptr, nullptr, nullptr, wg + n0, wu + n0, F_, epi);
}

struct EpiPart {
  float* part; int m0, n0;
  template <int NT, int NTHR>
  DI void run(f32x4 (&acc)[2][NT], char* lds, bool active, int row0, int fq) const {
    constexpr int RS = 592;
    if (active) {
#pragma unroll
      for (int m = 0; m < 2; ++m)
#pragma unroll
        for (int n = 0; n < NT; ++n)
          *reinterpret_cast<f32x4*>(lds + (row0 + m * 16) * RS + (n * 16 + fq * 4) * 4) = acc[m][n];
    }
    __syncthreads();
    for (int it = threadIdx.x; it < 128 * 36; it += NTHR) {
      const int row = it / 36, c = it % 36;
      f32x4 w = *reinterpret_cast<const f32x4*>(lds + row * RS + c * 16);
      *reinterpret_cast<f32x4*>(part + (size_t)(m0 + row) * D_ + n0 + c * 4) = w;
    }
  }
};

constexpr int LDS_DOWN = 4 * (128 * 64 * 2 + 64 * 144 * 2);
__global__ __launch_bounds__(512) void k_down(const u16* __restrict__ hh, const u16* __restrict__ wd,
                                               float* __restrict__ part) {
  extern __shared__ __attribute__((aligned(16))) char lds[];
  const int b = blockIdx.x, xcd = b & 7, idx = b >> 3;
  const int sk = xcd >> 1, bn = idx & 3, bm = (xcd & 1) * 8 + (idx >> 2);
  const int n0 = bn * 144;
  EpiPart epi{part + (size_t)sk * S_ * D_, bm * 128, n0};
  gemm_body<128, 144, 1, 4, 2, 64, 4, 0>(lds, hh, F_, bm * 128, sk * (F_ / 4), F_ / 4 / 64, nullptr, nullptr, nullptr, wd + n0, wd + n0, D_, epi);
}

__global__ __launch_bounds__(256) void k_final(const float* __restrict__ part, const u16* __restrict__ x1,
                                                const float* __restrict__ mod, u16* __restrict__ out) {
  const int i = (blockIdx.x * 256 + threadIdx.x) * 8;
  if (i >= S_ * D_) return;
  const int col = i % D_;
  const float* gb = mod + 5 * D_ + col;
  u32x4 xv = *reinterpret_cast<const u32x4*>(x1 + i);
  u32x4 o;
#pragma unroll
  for (int j = 0; j < 4; ++j) {
    float a = 0.f, b = 0.f;
#pragma unroll
    for (int s = 0; s < 4; ++s) {
      f32x2 p = *reinterpret_cast<const f32x2*>(part + (size_t)s * S_ * D_ + i + 2 * j);
      a += p[0]; b += p[1];
    }
    o[j] = pack2(bflo(xv[j]) + gb[2 * j] * a, bfhi(xv[j]) + gb[2 * j + 1] * b);
  }
  *reinterpret_cast<u32x4*>(out + i) = o;
}

extern "C" void kernel_launch(void* const* d_in, const int* in_sizes, int n_in,
                              void* d_out, int out_size, void* d_ws, size_t ws_size,
                              hipStream_t stream) {
  const u16* x = (const u16*)d_in[0];
  const u16* wqkv = (const u16*)d_in[1];
  const u16* wo = (const u16*)d_in[2];
  const u16* wg = (const u16*)d_in[4];
  const u16* wu = (const u16*)d_in[5];
  const u16* wd = (const u16*)d_in[6];
  const u16* c = (const u16*)d_in[7];
  const u16* wmod = (const u16*)d_in[8];
  char* ws = (char*)d_ws;
  float* mod = (float*)(ws + WS_MOD);
  float2* st1 = (float2*)(ws + WS_ST1);
  float2* st2 = (float2*)(ws + WS_ST2);
  u16* Q = (u16*)(ws + WS_Q);
  u16* K = (u16*)(ws + WS_K);
  u16* V = (u16*)(ws + WS_V);
  u16* O = (u16*)(ws + WS_O);
  u16* x1 = (u16*)(ws + WS_X1);
  u16* h2 = (u16*)(ws + WS_H2);
  u16* hh = (u16*)(ws + WS_HH);
  float* part = (float*)(ws + WS_PART);

  (void)hipFuncSetAttribute((const void*)k_qkv, hipFuncAttributeMaxDynamicSharedMemorySize, LDS_QKV);
  (void)hipFuncSetAttribute((const void*)k_proj, hipFuncAttributeMaxDynamicSharedMemorySize, LDS_PROJ);
  (void)hipFuncSetAttribute((const void*)k_glu, hipFuncAttributeMaxDynamicSharedMemorySize, LDS_GLU);
  (void)hipFuncSetAttribute((const void*)k_down, hipFuncAttributeMaxDynamicSharedMemorySize, LDS_DOWN);

  k_prep<<<108 + 512, 256, 0, stream>>>(c, wmod, x, mod, st1);
  k_qkv<<<192, 512, LDS_QKV, stream>>>(x, wqkv, st1, mod, Q);
  k_attn<<<32 * 16, 256, 0, stream>>>(Q, K, V, O);
  k_proj<<<256, 128, LDS_PROJ, stream>>>(O, wo, x, mod, x1, st2);
  k_ln2<<<512, 256, 0, stream>>>(x1, st2, mod, h2);
  k_glu<<<256, 512, LDS_GLU, stream>>>(h2, wg, wu, hh);
  k_down<<<256, 512, LDS_DOWN, stream>>>(hh, wd, part);
  k_final<<<(S_ * D_ / 8 + 255) / 256, 256, 0, stream>>>(part, x1, mod, (u16*)d_out);
}
```

```cpp
#include <hip/hip_runtime.h>
#include <hip/hip_bf16.h>
#include <stdint.h>
#include <type_traits>

typedef __attribute__((ext_vector_type(8))) short bf16x8;
typedef __attribute__((ext_vector_type(4))) short s16x4;
typedef __attribute__((ext_vector_type(4))) float f32x4;
typedef __attribute__((ext_vector_type(2))) float f32x2;
typedef __attribute__((ext_vector_type(16))) float f32x16;
typedef __attribute__((ext_vector_type(4))) unsigned u32x4;
typedef __attribute__((ext_vector_type(2))) unsigned u32x2;
typedef __attribute__((ext_vector_type(2))) __bf16 bf16x2_t;
typedef unsigned short u16;

#define DI __device__ __forceinline__

constexpr int S_ = 2048, D_ = 576, H_ = 32, DH_ = 18, F_ = 4608;
constexpr int HP = 32;
constexpr float QSCALE = 0.23570226039551584f * 1.4426950408889634f;

DI float bf2f(u16 v) { return __uint_as_float(((unsigned)v) << 16); }
DI float bflo(unsigned v) { return __uint_as_float(v << 16); }
DI float bfhi(unsigned v) { return __uint_as_float(v & 0xffff0000u); }
DI unsigned pack2(float a, float b) {
  f32x2 v = {a, b};
  bf16x2_t r = __builtin_convertvector(v, bf16x2_t);
  return __builtin_bit_cast(unsigned, r);
}
DI u16 f2bf(float a) { return (u16)(pack2(a, 0.f) & 0xffffu); }
DI float rbf(float a) { return bflo(pack2(a, 0.f) & 0xffffu); }

typedef __attribute__((address_space(3))) s16x4* lds_s16x4_ptr;
DI s16x4 tr_read(const char* p) {
  return __builtin_amdgcn_ds_read_tr16_b64_v4i16((lds_s16x4_ptr)(p));
}

constexpr size_t WS_MOD = 0;
constexpr size_t WS_ST1 = 16384;
constexpr size_t WS_ST2 = WS_ST1 + 2048 * 8;
constexpr size_t WS_Q = 1 << 20;
constexpr size_t WS_K = WS_Q + (size_t)H_ * S_ * HP * 2;
constexpr size_t WS_V = WS_K + (size_t)H_ * S_ * HP * 2;
constexpr size_t WS_O = WS_V + (size_t)H_ * S_ * HP * 2;
constexpr size_t WS_X1 = WS_O + (size_t)S_ * D_ * 2;
constexpr size_t WS_H2 = WS_X1 + (size_t)S_ * D_ * 2;
constexpr size_t WS_HH = WS_H2 + (size_t)S_ * D_ * 2;
constexpr size_t WS_PART = WS_HH + (size_t)S_ * F_ * 2;

__global__ __launch_bounds__(256) void k_prep(const u16* __restrict__ c, const u16* __restrict__ wmod,
                                               const u16* __restrict__ x, float* __restrict__ mod,
                                               float2* __restrict__ st1) {
  __shared__ float red[64][33];
  const int t = threadIdx.x;
  if (blockIdx.x < 108) {
    const int n0 = blockIdx.x * 32 + (t & 3) * 8;
    const int ks = t >> 2;
    float acc[8];
#pragma unroll
    for (int j = 0; j < 8; ++j) acc[j] = 0.f;
#pragma unroll
    for (int r = 0; r < 9; ++r) {
      const int k = ks * 9 + r;
      float cv = bf2f(c[k]);
      float sv = rbf(cv / (1.f + __expf(-cv)));
      u32x4 w = *reinterpret_cast<const u32x4*>(wmod + (size_t)k * 3456 + n0);
#pragma unroll
      for (int j = 0; j < 4; ++j) {
        acc[2 * j] += sv * bflo(w[j]);
        acc[2 * j + 1] += sv * bfhi(w[j]);
      }
    }
#pragma unroll
    for (int j = 0; j < 8; ++j) red[ks][(t & 3) * 8 + j] = acc[j];
    __syncthreads();
    if (t < 32) {
      float s = 0.f;
#pragma unroll 8
      for (int i = 0; i < 64; ++i) s += red[i][t];
      mod[blockIdx.x * 32 + t] = rbf(s);
    }
  } else {
    const int lane = t & 63, w = t >> 6;
    for (int row = (blockIdx.x - 108) * 4 + w; row < S_; row += (gridDim.x - 108) * 4) {
      const u16* xr = x + (size_t)row * D_;
      u32x4 a = *reinterpret_cast<const u32x4*>(xr + lane * 8);
      u32x4 b = {0, 0, 0, 0};
      if (lane < 8) b = *reinterpret_cast<const u32x4*>(xr + 512 + lane * 8);
      float v[16];
#pragma unroll
      for (int j = 0; j < 4; ++j) {
        v[2 * j] = bflo(a[j]); v[2 * j + 1] = bfhi(a[j]);
        v[8 + 2 * j] = bflo(b[j]); v[8 + 2 * j + 1] = bfhi(b[j]);
      }
      float s = 0.f;
#pragma unroll
      for (int j = 0; j < 16; ++j) s += v[j];
#pragma unroll
      for (int o = 32; o >= 1; o >>= 1) s += __shfl_xor(s, o);
      const float mean = s * (1.f / D_);
      float q = 0.f;
#pragma unroll
      for (int j = 0; j < 8; ++j) { float d = v[j] - mean; q += d * d; }
      if (lane < 8) {
#pragma unroll
        for (int j = 8; j < 16; ++j) { float d = v[j] - mean; q += d * d; }
      }
#pragma unroll
      for (int o = 32; o >= 1; o >>= 1) q += __shfl_xor(q, o);
      if (lane == 0) st1[row] = make_float2(mean, rsqrtf(q * (1.f / D_) + 1e-5f));
    }
  }
}

#define WAIT_LGKM0() asm volatile("s_waitcnt lgkmcnt(0)" ::: "memory")
#define RAW_BAR() __builtin_amdgcn_s_barrier()
template <int N> DI void wait_vm() { asm volatile("s_waitcnt vmcnt(%0)" ::"n"(N) : "memory"); }
template <int OFF> DI void lds_tr(u32x2& d, unsigned a) { asm volatile("ds_read_b64_tr_b16 %0, %1 offset:%2" : "=v"(d) : "v"(a), "n"(OFF)); }
template <int OFF> DI void lds_128(u32x4& d, unsigned a) { asm volatile("ds_read_b128 %0, %1 offset:%2" : "=v"(d) : "v"(a), "n"(OFF)); }
template <int N> DI void wait_l2(u32x2& a, u32x2& b) { asm volatile("s_waitcnt lgkmcnt(%2)" : "+v"(a), "+v"(b) : "n"(N)); }
template <int N> DI void wait_l4(u32x2& a, u32x2& b, u32x4& c, u32x4& d) { asm volatile("s_waitcnt lgkmcnt(%4)" : "+v"(a), "+v"(b), "+v"(c), "+v"(d) : "n"(N)); }

template <int BK> DI int a_swz(int row) { return BK == 64 ? ((row >> 1) & 7) : ((-(row >> 2)) & 3); }

template <int I, int N, class F> DI void static_for(F&& f) {
  if constexpr (I < N) { f(std::integral_constant<int, I>{}); static_for<I + 1, N>(f); }
}

template <int BM, int BN, int NIMG, int WM, int WK, int BK, int NST, int LNMODE  , class Epi>
DI void gemm_body(char* lds, const u16* __restrict__ A, int lda, int m0, int kbeg, int nk,
                  const float2* __restrict__ stats, const float* __restrict__ mscale, const float* __restrict__ mshift,
                  const u16* __restrict__ B0, const u16* __restrict__ B1, int ldb, Epi& epi) {
  constexpr int NTHR = 64 * WM * WK, NW = WM * WK;
  constexpr int NT = BN / 16, BNI = BN / NIMG, NTI = NT / NIMG;
  constexpr int ABYTES = BM * BK * 2, BBYTES = BK * BN * 2, STAGE = ABYTES + BBYTES;
  constexpr int AINS = ABYTES / 1024, BINS = BBYTES / 1024;
  constexpr int BINS_I = BINS / NIMG;
  constexpr int TINS = AINS + BINS;
  constexpr int G = (TINS + NW - 1) / NW;
  constexpr int SUB = BK / 32 / WK;
  constexpr int CPR = BK / 8;
  static_assert(BBYTES % (1024 * NIMG) == 0 && ABYTES % 1024 == 0 && (BK == 32 || BK == 64), "tile");
  const int t = threadIdx.x, lane = t & 63;
  const int wid = __builtin_amdgcn_readfirstlane(t >> 6);
  const int wr = wid % WM, wk = wid / WM, fr = lane & 15, fq = lane >> 4;
  float* lnp = reinterpret_cast<float*>(lds + NST * STAGE);

  unsigned goff[G];
#pragma unroll
  for (int ii = 0; ii < G; ++ii) {
    int i = wid + ii * NW;
    if (i >= TINS) i -= TINS;
    if (i < AINS) {
      const int j = i * 64 + lane;
      const int row = j / CPR, pos = j % CPR;
      goff[ii] = (unsigned)((row * lda + (pos ^ a_swz<BK>(row)) * 8) * 2);
    } else {
      const int j = ((i - AINS) % BINS_I) * 64 + lane;
      const int kb = j / BNI, rem = j % BNI;
      const int sub = rem >> 4, r = (rem >> 1) & 7, hf = rem & 1;
      goff[ii] = (unsigned)(((kb * 8 + (r ^ ((kb & 1) << 2))) * ldb + sub * 16 + hf * 8) * 2);
    }
  }
  const char* Abase = (const char*)(A + (size_t)m0 * lda + kbeg);
  const char* B0base = (const char*)(B0 + (size_t)kbeg * ldb);
  const char* B1base = (const char*)(B1 + (size_t)kbeg * ldb);

  auto stage_issue = [&](int kt) {
    char* buf = lds + (kt % NST) * STAGE;
    const size_t bk = (size_t)kt * BK * ldb * 2;
    const char* ab = Abase + kt * BK * 2;
#pragma unroll
    for (int ii = 0; ii < G; ++ii) {
      int i = wid + ii * NW;
      if (i >= TINS) i -= TINS;
      const char* base = (i < AINS) ? ab : (((NIMG == 2 && i - AINS >= BINS_I) ? B1base : B0base) + bk);
      __builtin_amdgcn_global_load_lds((const unsigned*)(base + goff[ii]),
                                       (__attribute__((address_space(3))) unsigned*)(buf + (i * 64 + lane) * 16), 16, 0, 0);
    }
  };

#pragma unroll
  for (int st = 0; st < NST - 1; ++st)
    if (st < nk) stage_issue(st);

  float ra[2], rb[2];
  if constexpr (LNMODE != 0) {
    for (int i = t; i < D_; i += NTHR) { lnp[i] = mscale[i] + 1.f; lnp[D_ + i] = mshift[i]; }
#pragma unroll
    for (int m = 0; m < 2; ++m) {
      float2 s = stats[m0 + wr * 32 + m * 16 + fr];
      ra[m] = s.y; rb[m] = -s.x * s.y;
    }
    wait_vm<0>();
    WAIT_LGKM0();
  }

  f32x4 acc[2][NT];
#pragma unroll
  for (int m = 0; m < 2; ++m)
#pragma unroll
    for (int n = 0; n < NT; ++n) acc[m][n] = f32x4{0.f, 0.f, 0.f, 0.f};

  const unsigned lbase = (unsigned)(size_t)lds;
  const int q4 = fr >> 2, p4 = lane & 3, sw = (fq & 1) << 2;
  const int arow0 = wr * 32 + fr, arow1 = arow0 + 16;
  const unsigned aoff0 = arow0 * (BK * 2), aoff1 = arow1 * (BK * 2);
  const int asw0 = a_swz<BK>(arow0), asw1 = a_swz<BK>(arow1);
  const unsigned boff_lo = ABYTES + (wk * 4 + fq) * (BNI * 16) + ((q4 ^ sw) << 5) + p4 * 8;
  const unsigned boff_hi = ABYTES + (wk * 4 + fq) * (BNI * 16) + (((q4 + 4) ^ sw) << 5) + p4 * 8;

  constexpr int PF = 4;
  constexpr int T = SUB * NT;
  for (int kt = 0; kt < nk; ++kt) {
    if (kt + NST - 2 < nk) wait_vm<G * (NST - 2)>(); else wait_vm<0>();
    RAW_BAR();
    if (kt + NST - 1 < nk) stage_issue(kt + NST - 1);
    const unsigned sb = lbase + (kt % NST) * STAGE;
    const unsigned vb_lo = sb + boff_lo, vb_hi = sb + boff_hi;
    u32x2 blo[PF], bhi[PF];
    u32x4 af[2][2];
    auto rda = [&](auto S2) {
      constexpr int s2 = decltype(S2)::value;
      const int ch = (wk + s2 * WK) * 4 + fq;
      lds_128<0>(af[s2 & 1][0], sb + aoff0 + ((ch ^ asw0) << 4));
      lds_128<0>(af[s2 & 1][1], sb + aoff1 + ((ch ^ asw1) << 4));
    };
    auto rdb = [&](auto IDX) {
      constexpr int idx = decltype(IDX)::value;
      constexpr int s2 = idx / NT, n = idx % NT;
      constexpr int off = s2 * WK * 4 * (BNI * 16) + (n / NTI) * (BBYTES / NIMG) + (n % NTI) * 256;
      lds_tr<off>(blo[idx % PF], vb_lo);
      lds_tr<off>(bhi[idx % PF], vb_hi);
    };
    rda(std::integral_constant<int, 0>{});
    static_for<0, PF - 1>([&](auto I) { rdb(I); });
    static_for<0, T>([&](auto IDX) {
      constexpr int idx = decltype(IDX)::value;
      constexpr int s2 = idx / NT, n = idx % NT;
      if constexpr (n == NT - PF && s2 + 1 < SUB) rda(std::integral_constant<int, s2 + 1>{});
      if constexpr (idx + PF - 1 < T) rdb(std::integral_constant<int, idx + PF - 1>{});
      constexpr int nb = ((idx + PF - 1 < T) ? (idx + PF - 1) : (T - 1)) - idx;
      constexpr int sA = (idx + PF) / NT - 1;
      constexpr int idxA = sA * NT + NT - PF;
      constexpr bool a_after = (sA >= 0) && (sA + 1 < SUB) && (idxA <= idx) && (idx <= idxA + PF - 2);
      constexpr int cnt = 2 * nb + (a_after ? 2 : 0);
      if constexpr (n == 0) {
        wait_l4<cnt>(blo[idx % PF], bhi[idx % PF], af[s2 & 1][0], af[s2 & 1][1]);
        if constexpr (LNMODE != 0) {
          const int kc = kbeg + kt * BK + (wk + s2 * WK) * 32 + fq * 8;
          f32x4 sc0 = *reinterpret_cast<const f32x4*>(lnp + kc), sc1 = *reinterpret_cast<const f32x4*>(lnp + kc + 4);
          f32x4 sh0 = *reinterpret_cast<const f32x4*>(lnp + D_ + kc), sh1 = *reinterpret_cast<const f32x4*>(lnp + D_ + kc + 4);
#pragma unroll
          for (int m = 0; m < 2; ++m) {
            u32x4 r = af[s2 & 1][m], o;
            const float a = ra[m], b = rb[m];
            o[0] = pack2((bflo(r[0]) * a + b) * sc0[0] + sh0[0], (bfhi(r[0]) * a + b) * sc0[1] + sh0[1]);
            o[1] = pack2((bflo(r[1]) * a + b) * sc0[2] + sh0[2], (bfhi(r[1]) * a + b) * sc0[3] + sh0[3]);
            o[2] = pack2((bflo(r[2]) * a + b) * sc1[0] + sh1[0], (bfhi(r[2]) * a + b) * sc1[1] + sh1[1]);
            o[3] = pack2((bflo(r[3]) * a + b) * sc1[2] + sh1[2], (bfhi(r[3]) * a + b) * sc1[3] + sh1[3]);
            af[s2 & 1][m] = o;
          }
        }
      } else {
        wait_l2<cnt>(blo[idx % PF], bhi[idx % PF]);
      }
      u32x4 bw = {blo[idx % PF][0], blo[idx % PF][1], bhi[idx % PF][0], bhi[idx % PF][1]};
      const bf16x8 bf = __builtin_bit_cast(bf16x8, bw);
      acc[0][n] = __builtin_amdgcn_mfma_f32_16x16x32_bf16(bf, __builtin_bit_cast(bf16x8, af[s2 & 1][0]), acc[0][n], 0, 0, 0);
      acc[1][n] = __builtin_amdgcn_mfma_f32_16x16x32_bf16(bf, __builtin_bit_cast(bf16x8, af[s2 & 1][1]), acc[1][n], 0, 0, 0);
    });
  }
  __syncthreads();
  if constexpr (WK > 1) {
    f32x4* red = reinterpret_cast<f32x4*>(lds);
    if (wk > 0) {
#pragma unroll
      for (int m = 0; m < 2; ++m)
#pragma unroll
        for (int n = 0; n < NT; ++n) red[(((wk - 1) * WM + wr) * 2 * NT + m * NT + n) * 64 + lane] = acc[m][n];
    }
    __syncthreads();
    if (wk == 0) {
#pragma unroll
      for (int g = 1; g < WK; ++g)
#pragma unroll
        for (int m = 0; m < 2; ++m)
#pragma unroll
          for (int n = 0; n < NT; ++n) acc[m][n] += red[(((g - 1) * WM + wr) * 2 * NT + m * NT + n) * 64 + lane];
    }
    __syncthreads();
  }
  epi.template run<NT, NTHR>(acc, lds, wk == 0, wr * 32 + fr, fq);
}

struct EpiQKV {
  u16* qkv;
  int m0, n0;
  template <int NT, int NTHR>
  DI void run(f32x4 (&acc)[2][NT], char* lds, bool active, int row0, int fq) const {
    constexpr int RS = 296;
    const int which = n0 / D_;
    const float sc = which == 0 ? QSCALE : 1.f;
    if (active) {
#pragma unroll
      for (int m = 0; m < 2; ++m)
#pragma unroll
        for (int n = 0; n < NT; ++n) {
          u32x2 o;
          o[0] = pack2(acc[m][n][0] * sc, acc[m][n][1] * sc);
          o[1] = pack2(acc[m][n][2] * sc, acc[m][n][3] * sc);
          *reinterpret_cast<u32x2*>(lds + (row0 + m * 16) * RS + (n * 16 + fq * 4) * 2) = o;
        }
    }
    __syncthreads();
    const int head0 = (n0 % D_) / DH_;
    const unsigned padw = (which == 2) ? 0x00003f80u : 0u;
    for (int it = threadIdx.x; it < 128 * 8; it += NTHR) {
      const int row = it >> 3, hd = it & 7;
      const unsigned* src = reinterpret_cast<const unsigned*>(lds + row * RS + hd * 36);
      u32x4 w0 = {src[0], src[1], src[2], src[3]};
      u32x4 w1 = {src[4], src[5], src[6], src[7]};
      u32x4 w2 = {src[8], padw, 0u, 0u};
      u32x4 w3 = {0u, 0u, 0u, 0u};
      u32x4* dst = reinterpret_cast<u32x4*>(qkv + ((size_t)(which * H_ + head0 + hd) * S_ + m0 + row) * HP);
      dst[0] = w0; dst[1] = w1; dst[2] = w2; dst[3] = w3;
    }
  }
};

constexpr int LDS_QKV = 4 * (128 * 64 * 2 + 64 * 144 * 2) + 2 * D_ * 4;
__global__ __launch_bounds__(512) void k_qkv(const u16* __restrict__ x, const u16* __restrict__ wqkv,
                                              const float2* __restrict__ st1, const float* __restrict__ mod,
                                              u16* __restrict__ qkv) {
  extern __shared__ __attribute__((aligned(16))) char lds[];
  const int b = blockIdx.x;
  const int xcd = b & 7, idx = b >> 3;
  const int bn = (idx % 3) * 4 + (xcd >> 1), bm = (idx / 3) * 2 + (xcd & 1);
  const int n0 = bn * 144;
  EpiQKV epi{qkv, bm * 128, n0};
  gemm_body<128, 144, 1, 4, 2, 64, 4, 1>(lds, x, D_, bm * 128, 0, D_ / 64, st1, mod + D_, mod, wqkv + n0, wqkv + n0, 3 * D_, epi);
}

#pragma float_control(precise, off, push)
__global__ __launch_bounds__(256, 2) void k_attn(const u16* __restrict__ Q, const u16* __restrict__ K,
                                                  const u16* __restrict__ V, u16* __restrict__ O) {
  __shared__ __attribute__((aligned(16))) char lds[2 * 16384];
  const int t = threadIdx.x, lane = t & 63, wid = t >> 6, r32 = lane & 31, h = lane >> 5;
  const int head = blockIdx.x >> 4, qb = blockIdx.x & 15;
  const int q0 = qb * 128 + wid * 32;
  const u16* Qh = Q + (size_t)head * S_ * HP;
  const u16* Kh = K + (size_t)head * S_ * HP;
  const u16* Vh = V + (size_t)head * S_ * HP;
  const bf16x8 qf0 = *reinterpret_cast<const bf16x8*>(Qh + (size_t)(q0 + r32) * HP + 8 * h);
  const bf16x8 qf1 = *reinterpret_cast<const bf16x8*>(Qh + (size_t)(q0 + r32) * HP + 16 + 8 * h);

  const int skey = t >> 2, sch = t & 3;
  const int kst_off = skey * 64 + ((sch ^ ((skey >> 2) & 3)) << 4);
  const int vst_off = 8192 + skey * 64 + sch * 16;
  u32x4 kreg0, kreg1, vreg0, vreg1;
  auto gload = [&](int st) {
    const u16* kp = Kh + (size_t)(st * 128 + skey) * HP + sch * 8;
    const u16* vp = Vh + (size_t)(st * 128 + skey) * HP + sch * 8;
    kreg0 = *reinterpret_cast<const u32x4*>(kp); kreg1 = *reinterpret_cast<const u32x4*>(kp + 64 * HP);
    vreg0 = *reinterpret_cast<const u32x4*>(vp); vreg1 = *reinterpret_cast<const u32x4*>(vp + 64 * HP);
  };
  auto swrite = [&](char* buf) {
    *reinterpret_cast<u32x4*>(buf + kst_off) = kreg0; *reinterpret_cast<u32x4*>(buf + kst_off + 4096) = kreg1;
    *reinterpret_cast<u32x4*>(buf + vst_off) = vreg0; *reinterpret_cast<u32x4*>(buf + vst_off + 4096) = vreg1;
  };

  f32x16 oacc, negm;
#pragma unroll
  for (int i = 0; i < 16; ++i) { oacc[i] = 0.f; negm[i] = 0.f; }
  constexpr float THR = 8.f;

  const int kr0 = r32 * 64 + (((0 + h) ^ ((r32 >> 2) & 3)) << 4);
  const int kr1 = r32 * 64 + (((2 + h) ^ ((r32 >> 2) & 3)) << 4);
  const int g = lane >> 4, q4 = (lane & 15) >> 2, p4 = lane & 3;
  const int vr = 8192 + (4 * (g >> 1) + q4) * 64 + (g & 1) * 32 + p4 * 8;

  gload(0);
  swrite(lds);
  __syncthreads();
  constexpr int NS = S_ / 128;
  for (int st = 0; st < NS; ++st) {
    const char* cur = lds + (st & 1) * 16384;
    char* nxt = lds + ((st + 1) & 1) * 16384;
    if (st + 1 < NS) gload(st + 1);
#pragma unroll
    for (int u = 0; u < 4; ++u) {
      const bf16x8 ka = *reinterpret_cast<const bf16x8*>(cur + u * 2048 + kr0);
      const bf16x8 kb = *reinterpret_cast<const bf16x8*>(cur + u * 2048 + kr1);
      f32x16 s = __builtin_amdgcn_mfma_f32_32x32x16_bf16(ka, qf0, negm, 0, 0, 0);
      s = __builtin_amdgcn_mfma_f32_32x32x16_bf16(kb, qf1, s, 0, 0, 0);
      float mx = fmaxf(s[0], s[1]);
#pragma unroll
      for (int i = 2; i < 16; ++i) mx = fmaxf(mx, s[i]);
      const bool first = (st == 0 && u == 0);
      if (first || !__all(mx <= THR)) {
        mx = fmaxf(mx, __shfl_xor(mx, 32));
        const float d = first ? mx : fmaxf(mx, 0.f);
        const float alpha = first ? 0.f : __builtin_amdgcn_exp2f(-d);
#pragma unroll
        for (int i = 0; i < 16; ++i) { oacc[i] *= alpha; negm[i] -= d; s[i] -= d; }
      }
      u32x4 pf0, pf1;
#pragma unroll
      for (int i = 0; i < 4; ++i) {
        pf0[i] = pack2(__builtin_amdgcn_exp2f(s[2 * i]), __builtin_amdgcn_exp2f(s[2 * i + 1]));
        pf1[i] = pack2(__builtin_amdgcn_exp2f(s[8 + 2 * i]), __builtin_amdgcn_exp2f(s[8 + 2 * i + 1]));
      }
      {
        s16x4 lo = tr_read(cur + vr + (32 * u) * 64);
        s16x4 hi = tr_read(cur + vr + (32 * u + 8) * 64);
        s16x4 lo2 = tr_read(cur + vr + (32 * u + 16) * 64);
        s16x4 hi2 = tr_read(cur + vr + (32 * u + 24) * 64);
        const bf16x8 vf0 = __builtin_shufflevector(lo, hi, 0, 1, 2, 3, 4, 5, 6, 7);
        const bf16x8 vf1 = __builtin_shufflevector(lo2, hi2, 0, 1, 2, 3, 4, 5, 6, 7);
        oacc = __builtin_amdgcn_mfma_f32_32x32x16_bf16(vf0, __builtin_bit_cast(bf16x8, pf0), oacc, 0, 0, 0);
        oacc = __builtin_amdgcn_mfma_f32_32x32x16_bf16(vf1, __builtin_bit_cast(bf16x8, pf1), oacc, 0, 0, 0);
      }
    }
    if (st + 1 < NS) swrite(nxt);
    __syncthreads();
  }
  const float l = __shfl(oacc[10], r32);
  const float inv = 1.f / l;
  unsigned* op = reinterpret_cast<unsigned*>(O + (size_t)(q0 + r32) * D_ + head * DH_);
  const unsigned w0 = pack2(oacc[0] * inv, oacc[1] * inv), w1 = pack2(oacc[2] * inv, oacc[3] * inv);
  const unsigned w2 = pack2(oacc[4] * inv, oacc[5] * inv), w3 = pack2(oacc[6] * inv, oacc[7] * inv);
  const unsigned w4 = pack2(oacc[8] * inv, oacc[9] * inv);
  op[2 * h] = w0; op[2 * h + 1] = w1; op[4 + 2 * h] = w2; op[5 + 2 * h] = w3;
  if (h == 0) op[8] = w4;
}
#pragma float_control(pop)

struct EpiProj {
  const u16* x; const float* ga; u16* x1; float2* st2; int m0, n0;
  template <int NT, int NTHR>
  DI void run(f32x4 (&acc)[2][NT], char* lds, bool active, int row0, int fq) const {
    if (!active) return;
#pragma unroll
    for (int m = 0; m < 2; ++m) {
      const int row = m0 + row0 + m * 16;
      float v[NT][4];
      float s = 0.f;
#pragma unroll
      for (int n = 0; n < NT; ++n) {
        const int col = n0 + n * 16 + fq * 4;
        u32x2 xv = *reinterpret_cast<const u32x2*>(x + (size_t)row * D_ + col);
        f32x4 g = *reinterpret_cast<const f32x4*>(ga + col);
        u32x2 o;
        o[0] = pack2(bflo(xv[0]) + g[0] * acc[m][n][0], bfhi(xv[0]) + g[1] * acc[m][n][1]);
        o[1] = pack2(bflo(xv[1]) + g[2] * acc[m][n][2], bfhi(xv[1]) + g[3] * acc[m][n][3]);
        *reinterpret_cast<u32x2*>(x1 + (size_t)row * D_ + col) = o;
        v[n][0] = bflo(o[0]); v[n][1] = bfhi(o[0]); v[n][2] = bflo(o[1]); v[n][3] = bfhi(o[1]);
        s += (v[n][0] + v[n][1]) + (v[n][2] + v[n][3]);
      }
      s += __shfl_xor(s, 16); s += __shfl_xor(s, 32);
      const float lm = s * (1.f / (16 * NT));
      float q = 0.f;
#pragma unroll
      for (int n = 0; n < NT; ++n)
#pragma unroll
        for (int j = 0; j < 4; ++j) { float d = v[n][j] - lm; q += d * d; }
      q += __shfl_xor(q, 16); q += __shfl_xor(q, 32);
      if (fq == 0) st2[(size_t)row * 4 + n0 / 144] = make_float2(s, q);
    }
  }
};

constexpr int LDS_PROJ = 4 * (32 * 64 * 2 + 64 * 144 * 2);
__global__ __launch_bounds__(128) void k_proj(const u16* __restrict__ o, const u16* __restrict__ wo,
                                               const u16* __restrict__ x, const float* __restrict__ mod,
                                               u16* __restrict__ x1, float2* __restrict__ st2) {
  extern __shared__ __attribute__((aligned(16))) char lds[];
  const int b = blockIdx.x;
  const int bn = b & 3, bm = b >> 2;
  const int n0 = bn * 144;
  EpiProj epi{x, mod + 2 * D_, x1, st2, bm * 32, n0};
  gemm_body<32, 144, 1, 1, 2, 64, 4, 0>(lds, o, D_, bm * 32, 0, D_ / 64, nullptr, nullptr, nullptr, wo + n0, wo + n0, D_, epi);
}

__global__ __launch_bounds__(256) void k_ln2(const u16* __restrict__ x1, const float2* __restrict__ st2,
                                              const float* __restrict__ mod, u16* __restrict__ h2) {
  const int row = blockIdx.x * 4 + (threadIdx.x >> 6), lane = threadIdx.x & 63;
  const float2* p = st2 + (size_t)row * 4;
  float2 p0 = p[0], p1 = p[1], p2 = p[2], p3 = p[3];
  const float mean = (p0.x + p1.x + p2.x + p3.x) * (1.f / D_);
  float d0 = p0.x * (1.f / 144.f) - mean, d1 = p1.x * (1.f / 144.f) - mean, d2 = p2.x * (1.f / 144.f) - mean, d3 = p3.x * (1.f / 144.f) - mean;
  float m2 = p0.y + p1.y + p2.y + p3.y + 144.f * (d0 * d0 + d1 * d1 + d2 * d2 + d3 * d3);
  const float a = rsqrtf(m2 * (1.f / D_) + 1e-5f), b = -mean * a;
  const float* sb = mod + 3 * D_;
  const float* cb = mod + 4 * D_;
  for (int c = lane; c < 72; c += 64) {
    u32x4 r = *reinterpret_cast<const u32x4*>(x1 + (size_t)row * D_ + c * 8);
    f32x4 sc0 = *reinterpret_cast<const f32x4*>(cb + c * 8), sc1 = *reinterpret_cast<const f32x4*>(cb + c * 8 + 4);
    f32x4 sh0 = *reinterpret_cast<const f32x4*>(sb + c * 8), sh1 = *reinterpret_cast<const f32x4*>(sb + c * 8 + 4);
    u32x4 o;
    o[0] = pack2((bflo(r[0]) * a + b) * (sc0[0] + 1.f) + sh0[0], (bfhi(r[0]) * a + b) * (sc0[1] + 1.f) + sh0[1]);
    o[1] = pack2((bflo(r[1]) * a + b) * (sc0[2] + 1.f) + sh0[2], (bfhi(r[1]) * a + b) * (sc0[3] + 1.f) + sh0[3]);
    o[2] = pack2((bflo(r[2]) * a + b) * (sc1[0] + 1.f) + sh1[0], (bfhi(r[2]) * a + b) * (sc1[1] + 1.f) + sh1[1]);
    o[3] = pack2((bflo(r[3]) * a + b) * (sc1[2] + 1.f) + sh1[2], (bfhi(r[3]) * a + b) * (sc1[3] + 1.f) + sh1[3]);
    *reinterpret_cast<u32x4*>(h2 + (size_t)row * D_ + c * 8) = o;
  }
}

struct EpiGLU {
  u16* hh; int m0, n0;
  template <int NT, int NTHR>
  DI void run(f32x4 (&acc)[2][NT], char* lds, bool active, int row0, int fq) const {
    constexpr int RS = 296;
#pragma unroll
    for (int m = 0; m < 2; ++m)
#pragma unroll
      for (int n = 0; n < NT / 2; ++n) {
        u32x2 o;
        o[0] = pack2(acc[m][n][0] * acc[m][n + NT / 2][0], acc[m][n][1] * acc[m][n + NT / 2][1]);
        o[1] = pack2(acc[m][n][2] * acc[m][n + NT / 2][2], acc[m][n][3] * acc[m][n + NT / 2][3]);
        *reinterpret_cast<u32x2*>(lds + (row0 + m * 16) * RS + (n * 16 + fq * 4) * 2) = o;
      }
    __syncthreads();
    for (int it = threadIdx.x; it < 256 * 18; it += NTHR) {
      const int row = it / 18, c = it % 18;
      const u32x2* src = reinterpret_cast<const u32x2*>(lds + row * RS + c * 16);
      u32x2 lo = src[0], hi = src[1];
      u32x4 w = {lo[0], lo[1], hi[0], hi[1]};
      *reinterpret_cast<u32x4*>(hh + (size_t)(m0 + row) * F_ + n0 + c * 8) = w;
    }
  }
};

constexpr int LDS_GLU = 4 * (256 * 32 * 2 + 32 * 288 * 2);
__global__ __launch_bounds__(512) void k_glu(const u16* __restrict__ h2, const u16* __restrict__ wg,
                                              const u16* __restrict__ wu, u16* __restrict__ hh) {
  extern __shared__ __attribute__((aligned(16))) char lds[];
  const int b = blockIdx.x, xcd = b & 7, idx = b >> 3;
  const int bm = idx & 7, bn = xcd * 4 + (idx >> 3);
  const int n0 = bn * 144;
  EpiGLU epi{hh, bm * 256, n0};
  gemm_body<256, 288, 2, 8, 1, 32, 4, 0>(lds, h2, D_, bm * 256, 0, D_ / 32, nullptr, nullptr, nullptr, wg + n0, wu + n0, F_, epi);
}

struct EpiPart {
  float* part; int m0, n0;
  template <int NT, int NTHR>
  DI void run(f32x4 (&acc)[2][NT], char* lds, bool active, int row0, int fq) const {
    constexpr int RS = 592;
    if (active) {
#pragma unroll
      for (int m = 0; m < 2; ++m)
#pragma unroll
        for (int n = 0; n < NT; ++n)
          *reinterpret_cast<f32x4*>(lds + (row0 + m * 16) * RS + (n * 16 + fq * 4) * 4) = acc[m][n];
    }
    __syncthreads();
    for (int it = threadIdx.x; it < 128 * 36; it += NTHR) {
      const int row = it / 36, c = it % 36;
      f32x4 w = *reinterpret_cast<const f32x4*>(lds + row * RS + c * 16);
      *reinterpret_cast<f32x4*>(part + (size_t)(m0 + row) * D_ + n0 + c * 4) = w;
    }
  }
};

constexpr int LDS_DOWN = 4 * (128 * 64 * 2 + 64 * 144 * 2);
__global__ __launch_bounds__(512) void k_down(const u16* __restrict__ hh, const u16* __restrict__ wd,
                                               float* __restrict__ part) {
  extern __shared__ __attribute__((aligned(16))) char lds[];
  const int b = blockIdx.x, xcd = b & 7, idx = b >> 3;
  const int sk = xcd >> 1, bn = idx & 3, bm = (xcd & 1) * 8 + (idx >> 2);
  const int n0 = bn * 144;
  EpiPart epi{part + (size_t)sk * S_ * D_, bm * 128, n0};
  gemm_body<128, 144, 1, 4, 2, 64, 4, 0>(lds, hh, F_, bm * 128, sk * (F_ / 4), F_ / 4 / 64, nullptr, nullptr, nullptr, wd + n0, wd + n0, D_, epi);
}

__global__ __launch_bounds__(256) void k_final(const float* __restrict__ part, const u16* __restrict__ x1,
                                                const float* __restrict__ mod, u16* __restrict__ out) {
  const int i = (blockIdx.x * 256 + threadIdx.x) * 8;
  if (i >= S_ * D_) return;
  const int col = i % D_;
  const float* gb = mod + 5 * D_ + col;
  u32x4 xv = *reinterpret_cast<const u32x4*>(x1 + i);
  u32x4 o;
#pragma unroll
  for (int j = 0; j < 4; ++j) {
    float a = 0.f, b = 0.f;
#pragma unroll
    for (int s = 0; s < 4; ++s) {
      f32x2 p = *reinterpret_cast<const f32x2*>(part + (size_t)s * S_ * D_ + i + 2 * j);
      a += p[0]; b += p[1];
    }
    o[j] = pack2(bflo(xv[j]) + gb[2 * j] * a, bfhi(xv[j]) + gb[2 * j + 1] * b);
  }
  *reinterpret_cast<u32x4*>(out + i) = o;
}

extern "C" void kernel_launch(void* const* d_in, const int* in_sizes, int n_in,
                              void* d_out, int out_size, void* d_ws, size_t ws_size,
                              hipStream_t stream) {
  const u16* x = (const u16*)d_in[0];
  const u16* wqkv = (const u16*)d_in[1];
  const u16* wo = (const u16*)d_in[2];
  const u16* wg = (const u16*)d_in[4];
  const u16* wu = (const u16*)d_in[5];
  const u16* wd = (const u16*)d_in[6];
  const u16* c = (const u16*)d_in[7];
  const u16* wmod = (const u16*)d_in[8];
  char* ws = (char*)d_ws;
  float* mod = (float*)(ws + WS_MOD);
  float2* st1 = (float2*)(ws + WS_ST1);
  float2* st2 = (float2*)(ws + WS_ST2);
  u16* Q = (u16*)(ws + WS_Q);
  u16* K = (u16*)(ws + WS_K);
  u16* V = (u16*)(ws + WS_V);
  u16* O = (u16*)(ws + WS_O);
  u16* x1 = (u16*)(ws + WS_X1);
  u16* h2 = (u16*)(ws + WS_H2);
  u16* hh = (u16*)(ws + WS_HH);
  float* part = (float*)(ws + WS_PART);

  (void)hipFuncSetAttribute((const void*)k_qkv, hipFuncAttributeMaxDynamicSharedMemorySize, LDS_QKV);
  (void)hipFuncSetAttribute((const void*)k_proj, hipFuncAttributeMaxDynamicSharedMemorySize, LDS_PROJ);
  (void)hipFuncSetAttribute((const void*)k_glu, hipFuncAttributeMaxDynamicSharedMemorySize, LDS_GLU);
  (void)hipFuncSetAttribute((const void*)k_down, hipFuncAttributeMaxDynamicSharedMemorySize, LDS_DOWN);

  k_prep<<<108 + 512, 256, 0, stream>>>(c, wmod, x, mod, st1);
  k_qkv<<<192, 512, LDS_QKV, stream>>>(x, wqkv, st1, mod, Q);
  k_attn<<<32 * 16, 256, 0, stream>>>(Q, K, V, O);
  k_proj<<<256, 128, LDS_PROJ, stream>>>(O, wo, x, mod, x1, st2);
  k_ln2<<<512, 256, 0, stream>>>(x1, st2, mod, h2);
  k_glu<<<256, 512, LDS_GLU, stream>>>(h2, wg, wu, hh);
  k_down<<<256, 512, LDS_DOWN, stream>>>(hh, wd, part);
  k_final<<<(S_ * D_ / 8 + 255) / 256, 256, 0, stream>>>(part, x1, mod, (u16*)d_out);
}
```

```cpp
#include <hip/hip_runtime.h>
#include <hip/hip_bf16.h>
#include <stdint.h>
#include <type_traits>

typedef __attribute__((ext_vector_type(8))) short bf16x8;
typedef __attribute__((ext_vector_type(4))) short s16x4;
typedef __attribute__((ext_vector_type(4))) float f32x4;
typedef __attribute__((ext_vector_type(2))) float f32x2;
typedef __attribute__((ext_vector_type(16))) float f32x16;
typedef __attribute__((ext_vector_type(4))) unsigned u32x4;
typedef __attribute__((ext_vector_type(2))) unsigned u32x2;
typedef __attribute__((ext_vector_type(2))) __bf16 bf16x2_t;
typedef unsigned short u16;

#define DI __device__ __forceinline__

constexpr int S_ = 2048, D_ = 576, H_ = 32, DH_ = 18, F_ = 4608;
constexpr int HP = 32;
constexpr float QSCALE = 0.23570226039551584f * 1.4426950408889634f;

DI float bf2f(u16 v) { return __uint_as_float(((unsigned)v) << 16); }
DI float bflo(unsigned v) { return __uint_as_float(v << 16); }
DI float bfhi(unsigned v) { return __uint_as_float(v & 0xffff0000u); }
DI unsigned pack2(float a, float b) {
  f32x2 v = {a, b};
  bf16x2_t r = __builtin_convertvector(v, bf16x2_t);
  return __builtin_bit_cast(unsigned, r);
}
DI u16 f2bf(float a) { return (u16)(pack2(a, 0.f) & 0xffffu); }
DI float rbf(float a) { return bflo(pack2(a, 0.f) & 0xffffu); }

typedef __attribute__((address_space(3))) s16x4* lds_s16x4_ptr;
DI s16x4 tr_read(const char* p) {
  return __builtin_amdgcn_ds_read_tr16_b64_v4i16((lds_s16x4_ptr)(p));
}

constexpr size_t WS_MOD = 0;
constexpr size_t WS_ST1 = 16384;
constexpr size_t WS_ST2 = WS_ST1 + 2048 * 8;
constexpr size_t WS_Q = 1 << 20;
constexpr size_t WS_K = WS_Q + (size_t)H_ * S_ * HP * 2;
constexpr size_t WS_V = WS_K + (size_t)H_ * S_ * HP * 2;
constexpr size_t WS_O = WS_V + (size_t)H_ * S_ * HP * 2;
constexpr size_t WS_X1 = WS_O + (size_t)S_ * D_ * 2;
constexpr size_t WS_H2 = WS_X1 + (size_t)S_ * D_ * 2;
constexpr size_t WS_HH = WS_H2 + (size_t)S_ * D_ * 2;
constexpr size_t WS_PART = WS_HH + (size_t)S_ * F_ * 2;

__global__ __launch_bounds__(256) void k_prep(const u16* __restrict__ c, const u16* __restrict__ wmod,
                                               const u16* __restrict__ x, float* __restrict__ mod,
                                               float2* __restrict__ st1) {
  __shared__ float red[64][33];
  const int t = threadIdx.x;
  if (blockIdx.x < 108) {
    const int n0 = blockIdx.x * 32 + (t & 3) * 8;
    const int ks = t >> 2;
    float acc[8];
#pragma unroll
    for (int j = 0; j < 8; ++j) acc[j] = 0.f;
#pragma unroll
    for (int r = 0; r < 9; ++r) {
      const int k = ks * 9 + r;
      float cv = bf2f(c[k]);
      float sv = rbf(cv / (1.f + __expf(-cv)));
      u32x4 w = *reinterpret_cast<const u32x4*>(wmod + (size_t)k * 3456 + n0);
#pragma unroll
      for (int j = 0; j < 4; ++j) {
        acc[2 * j] += sv * bflo(w[j]);
        acc[2 * j + 1] += sv * bfhi(w[j]);
      }
    }
#pragma unroll
    for (int j = 0; j < 8; ++j) red[ks][(t & 3) * 8 + j] = acc[j];
    __syncthreads();
    if (t < 32) {
      float s = 0.f;
#pragma unroll 8
      for (int i = 0; i < 64; ++i) s += red[i][t];
      mod[blockIdx.x * 32 + t] = rbf(s);
    }
  } else {
    const int lane = t & 63, w = t >> 6;
    for (int row = (blockIdx.x - 108) * 4 + w; row < S_; row += (gridDim.x - 108) * 4) {
      const u16* xr = x + (size_t)row * D_;
      u32x4 a = *reinterpret_cast<const u32x4*>(xr + lane * 8);
      u32x4 b = {0, 0, 0, 0};
      if (lane < 8) b = *reinterpret_cast<const u32x4*>(xr + 512 + lane * 8);
      float v[16];
#pragma unroll
      for (int j = 0; j < 4; ++j) {
        v[2 * j] = bflo(a[j]); v[2 * j + 1] = bfhi(a[j]);
        v[8 + 2 * j] = bflo(b[j]); v[8 + 2 * j + 1] = bfhi(b[j]);
      }
      float s = 0.f;
#pragma unroll
      for (int j = 0; j < 16; ++j) s += v[j];
#pragma unroll
      for (int o = 32; o >= 1; o >>= 1) s += __shfl_xor(s, o);
      const float mean = s * (1.f / D_);
      float q = 0.f;
#pragma unroll
      for (int j = 0; j < 8; ++j) { float d = v[j] - mean; q += d * d; }
      if (lane < 8) {
#pragma unroll
        for (int j = 8; j < 16; ++j) { float d = v[j] - mean; q += d * d; }
      }
#pragma unroll
      for (int o = 32; o >= 1; o >>= 1) q += __shfl_xor(q, o);
      if (lane == 0) st1[row] = make_float2(mean, rsqrtf(q * (1.f / D_) + 1e-5f));
    }
  }
}

#define WAIT_LGKM0() asm volatile("s_waitcnt lgkmcnt(0)" ::: "memory")
#define RAW_BAR() __builtin_amdgcn_s_barrier()
template <int N> DI void wait_vm() { asm volatile("s_waitcnt vmcnt(%0)" ::"n"(N) : "memory"); }
template <int OFF> DI void lds_tr(u32x2& d, unsigned a) { asm volatile("ds_read_b64_tr_b16 %0, %1 offset:%2" : "=v"(d) : "v"(a), "n"(OFF)); }
template <int OFF> DI void lds_128(u32x4& d, unsigned a) { asm volatile("ds_read_b128 %0, %1 offset:%2" : "=v"(d) : "v"(a), "n"(OFF)); }
DI void pin4(u32x4& a) { asm volatile("" : "+v"(a)); }
template <int N> DI void wait_l2(u32x2& a, u32x2& b) { asm volatile("s_waitcnt lgkmcnt(%2)" : "+v"(a), "+v"(b) : "n"(N)); }
template <int N> DI void wait_l4(u32x2& a, u32x2& b, u32x4& c, u32x4& d) { asm volatile("s_waitcnt lgkmcnt(%4)" : "+v"(a), "+v"(b), "+v"(c), "+v"(d) : "n"(N)); }

template <int BK> DI int a_swz(int row) { return BK >= 64 ? ((row >> 1) & 7) : ((-(row >> 2)) & 3); }

template <int I, int N, class F> DI void static_for(F&& f) {
  if constexpr (I < N) { f(std::integral_constant<int, I>{}); static_for<I + 1, N>(f); }
}

template <int BM, int BN, int NIMG, int MT, int WM, int WN, int WK, int BK, int NST, int LNMODE  , class Epi>
DI void gemm_body(char* lds, const u16* __restrict__ A, int lda, int m0, int kbeg, int nk,
                  const float2* __restrict__ stats, const float* __restrict__ mscale, const float* __restrict__ mshift,
                  const u16* __restrict__ B0, const u16* __restrict__ B1, int ldb, Epi& epi) {
  constexpr int NW = WM * WN * WK, NTHR = 64 * NW;
  constexpr int NT = BN / 16, NTW = NT / WN, BNI = BN / NIMG, NTI = NT / NIMG;
  constexpr int ABYTES = BM * BK * 2, BBYTES = BK * BN * 2, STAGE = ABYTES + BBYTES;
  constexpr int AINS = ABYTES / 1024, BINS = BBYTES / 1024;
  constexpr int BINS_I = BINS / NIMG;
  constexpr int TINS = AINS + BINS;
  constexpr int G = (TINS + NW - 1) / NW;
  constexpr int SUB = BK / 32 / WK;
  constexpr int CPR = BK >= 64 ? 8 : 4;
  constexpr int ARB = CPR * 16;
  static_assert(BBYTES % (1024 * NIMG) == 0 && ABYTES % 1024 == 0 && BM == WM * MT * 16 && NT % WN == 0, "tile");
  static_assert(WN == 1 || NTW % NTI == 0 || NTI % NTW == 0, "img");
  const int t = threadIdx.x, lane = t & 63;
  const int wid = __builtin_amdgcn_readfirstlane(t >> 6);
  const int wr = wid % WM, wn = (wid / WM) % WN, wk = wid / (WM * WN), fr = lane & 15, fq = lane >> 4;
  float* lnp = reinterpret_cast<float*>(lds + NST * STAGE);

  unsigned goff[G];
#pragma unroll
  for (int ii = 0; ii < G; ++ii) {
    int i = wid + ii * NW;
    if (i >= TINS) i -= TINS;
    if (i < AINS) {
      const int j = i * 64 + lane;
      const int si = j / (BM * CPR), row = (j / CPR) % BM, pos = j % CPR;
      goff[ii] = (unsigned)((row * lda + si * 64 + (pos ^ a_swz<BK>(row)) * 8) * 2);
    } else {
      const int j = ((i - AINS) % BINS_I) * 64 + lane;
      const int kb = j / BNI, rem = j % BNI;
      const int sub = rem >> 4, r = (rem >> 1) & 7, hf = rem & 1;
      goff[ii] = (unsigned)(((kb * 8 + (r ^ ((kb & 1) << 2))) * ldb + sub * 16 + hf * 8) * 2);
    }
  }
  const char* Abase = (const char*)(A + (size_t)m0 * lda + kbeg);
  const char* B0base = (const char*)(B0 + (size_t)kbeg * ldb);
  const char* B1base = (const char*)(B1 + (size_t)kbeg * ldb);

  auto issue_one = [&](int kt, int ii) {
    char* buf = lds + (kt % NST) * STAGE;
    int i = wid + ii * NW;
    if (i >= TINS) i -= TINS;
    const char* base = (i < AINS) ? (Abase + kt * BK * 2)
                                  : (((NIMG == 2 && i - AINS >= BINS_I) ? B1base : B0base) + (size_t)kt * BK * ldb * 2);
    __builtin_amdgcn_global_load_lds((const unsigned*)(base + goff[ii]),
                                     (__attribute__((address_space(3))) unsigned*)(buf + (i * 64 + lane) * 16), 16, 0, 0);
  };

#pragma unroll
  for (int st = 0; st < NST - 1; ++st)
    if (st < nk) {
#pragma unroll
      for (int ii = 0; ii < G; ++ii) issue_one(st, ii);
    }

  float ra[MT], rb[MT];
  if constexpr (LNMODE != 0) {
    for (int i = t; i < D_; i += NTHR) { lnp[i] = mscale[i] + 1.f; lnp[D_ + i] = mshift[i]; }
#pragma unroll
    for (int m = 0; m < MT; ++m) {
      float2 s = stats[m0 + (wr * MT + m) * 16 + fr];
      ra[m] = s.y; rb[m] = -s.x * s.y;
    }
    wait_vm<0>();
    WAIT_LGKM0();
  }

  f32x4 acc[MT][NTW];
#pragma unroll
  for (int m = 0; m < MT; ++m)
#pragma unroll
    for (int n = 0; n < NTW; ++n) acc[m][n] = f32x4{0.f, 0.f, 0.f, 0.f};

  const unsigned lbase = (unsigned)(size_t)lds;
  const int q4 = fr >> 2, p4 = lane & 3, sw = (fq & 1) << 2;
  const int arow = wr * MT * 16 + fr;
  const unsigned aoffb = arow * ARB;
  const int tile0 = wn * NTW;
  const unsigned bimg = (unsigned)((tile0 / NTI) * (BBYTES / NIMG) + (tile0 % NTI) * 256);
  const unsigned boff_lo = ABYTES + bimg + (wk * 4 + fq) * (BNI * 16) + ((q4 ^ sw) << 5) + p4 * 8;
  const unsigned boff_hi = ABYTES + bimg + (wk * 4 + fq) * (BNI * 16) + (((q4 + 4) ^ sw) << 5) + p4 * 8;

  constexpr int PF = 3;
  constexpr int T = SUB * NTW;
  for (int kt = 0; kt < nk; ++kt) {
    if (kt + NST - 2 < nk) wait_vm<G * (NST - 2)>(); else wait_vm<0>();
    RAW_BAR();
    const bool more = kt + NST - 1 < nk;
    const unsigned sb = lbase + (kt % NST) * STAGE;
    const unsigned vb_lo = sb + boff_lo, vb_hi = sb + boff_hi;
    u32x2 blo[PF], bhi[PF];
    u32x4 af[2][MT];
    auto rda = [&](auto S2) {
      constexpr int s2 = decltype(S2)::value;
      const int ss = wk + s2 * WK;
      const int ch = (ss & (CPR / 4 - 1)) * 4 + fq;
      const unsigned base = sb + (ss / (CPR / 4)) * (BM * ARB) + aoffb;
      static_for<0, MT>([&](auto M) {
        constexpr int m = decltype(M)::value;
        lds_128<m * 16 * ARB>(af[s2 & 1][m], base + ((ch ^ a_swz<BK>(arow + m * 16)) << 4));
      });
    };
    auto rdb = [&](auto IDX) {
      constexpr int idx = decltype(IDX)::value;
      constexpr int s2 = idx / NTW, n = idx % NTW;
      constexpr int off = s2 * WK * 4 * (BNI * 16) + (NTW <= NTI ? n * 256 : (n / NTI) * (BBYTES / NIMG) + (n % NTI) * 256);
      lds_tr<off>(blo[idx % PF], vb_lo);
      lds_tr<off>(bhi[idx % PF], vb_hi);
    };
    rda(std::integral_constant<int, 0>{});
    static_for<0, PF - 1>([&](auto I) { rdb(I); });
    static_for<0, T>([&](auto IDX) {
      constexpr int idx = decltype(IDX)::value;
      constexpr int s2 = idx / NTW, n = idx % NTW;
      if constexpr (n == NTW - PF && s2 + 1 < SUB) rda(std::integral_constant<int, s2 + 1>{});
      if constexpr (idx + PF - 1 < T) rdb(std::integral_constant<int, idx + PF - 1>{});
      constexpr int nb = ((idx + PF - 1 < T) ? (idx + PF - 1) : (T - 1)) - idx;
      constexpr int sA = (idx + PF) / NTW - 1;
      constexpr int idxA = sA * NTW + NTW - PF;
      constexpr bool a_after = (sA >= 0) && (sA + 1 < SUB) && (idxA <= idx) && (idx <= idxA + PF - 2);
      constexpr int cnt = 2 * nb + (a_after ? MT : 0);
      if constexpr (n == 0) {
        asm volatile("s_waitcnt lgkmcnt(%0)" ::"n"(cnt));
        pin4(af[s2 & 1][0]); pin4(af[s2 & 1][1]);
        if constexpr (MT > 2) { pin4(af[s2 & 1][MT - 2]); pin4(af[s2 & 1][MT - 1]); }
        asm volatile("" : "+v"(blo[idx % PF]), "+v"(bhi[idx % PF]));
        if constexpr (LNMODE != 0) {
          const int kc = kbeg + kt * BK + (wk + s2 * WK) * 32 + fq * 8;
          f32x4 sc0 = *reinterpret_cast<const f32x4*>(lnp + kc), sc1 = *reinterpret_cast<const f32x4*>(lnp + kc + 4);
          f32x4 sh0 = *reinterpret_cast<const f32x4*>(lnp + D_ + kc), sh1 = *reinterpret_cast<const f32x4*>(lnp + D_ + kc + 4);
#pragma unroll
          for (int m = 0; m < MT; ++m) {
            u32x4 r = af[s2 & 1][m], o;
            const float a = ra[m], b = rb[m];
            o[0] = pack2((bflo(r[0]) * a + b) * sc0[0] + sh0[0], (bfhi(r[0]) * a + b) * sc0[1] + sh0[1]);
            o[1] = pack2((bflo(r[1]) * a + b) * sc0[2] + sh0[2], (bfhi(r[1]) * a + b) * sc0[3] + sh0[3]);
            o[2] = pack2((bflo(r[2]) * a + b) * sc1[0] + sh1[0], (bfhi(r[2]) * a + b) * sc1[1] + sh1[1]);
            o[3] = pack2((bflo(r[3]) * a + b) * sc1[2] + sh1[2], (bfhi(r[3]) * a + b) * sc1[3] + sh1[3]);
            af[s2 & 1][m] = o;
          }
        }
      } else {
        wait_l2<cnt>(blo[idx % PF], bhi[idx % PF]);
      }
      u32x4 bw = {blo[idx % PF][0], blo[idx % PF][1], bhi[idx % PF][0], bhi[idx % PF][1]};
      const bf16x8 bf = __builtin_bit_cast(bf16x8, bw);
#pragma unroll
      for (int m = 0; m < MT; ++m) {
        acc[m][n] = __builtin_amdgcn_mfma_f32_16x16x32_bf16(bf, __builtin_bit_cast(bf16x8, af[s2 & 1][m]), acc[m][n], 0, 0, 0);
      }
      static_for<0, G>([&](auto II) {
        constexpr int ii = decltype(II)::value;
        if constexpr (idx == (ii * T) / G) { if (more) issue_one(kt + NST - 1, ii); }
      });
    });
  }
  __syncthreads();
  if constexpr (WK > 1) {
    f32x4* red = reinterpret_cast<f32x4*>(lds);
    constexpr int PER = MT * NTW * 64;
#pragma unroll
    for (int step = WK / 2; step >= 1; step >>= 1) {
      if (wk >= step && wk < 2 * step) {
#pragma unroll
        for (int m = 0; m < MT; ++m)
#pragma unroll
          for (int n = 0; n < NTW; ++n) red[((wk - step) * WM * WN + wn * WM + wr) * PER + (m * NTW + n) * 64 + lane] = acc[m][n];
      }
      __syncthreads();
      if (wk < step) {
#pragma unroll
        for (int m = 0; m < MT; ++m)
#pragma unroll
          for (int n = 0; n < NTW; ++n) acc[m][n] += red[(wk * WM * WN + wn * WM + wr) * PER + (m * NTW + n) * 64 + lane];
      }
      __syncthreads();
    }
  }
  epi.template run<MT, NTW, NTHR>(acc, lds, wk == 0, wr * MT * 16 + fr, wn, fq);
}

struct EpiQKV {
  u16* qkv;
  int m0, n0;
  template <int MT, int NT, int NTHR>
  DI void run(f32x4 (&acc)[MT][NT], char* lds, bool active, int row0, int wn, int fq) const {
    constexpr int RS = 296;
    const int which = n0 / D_;
    const float sc = which == 0 ? QSCALE : 1.f;
    if (active) {
#pragma unroll
      for (int m = 0; m < MT; ++m)
#pragma unroll
        for (int n = 0; n < NT; ++n) {
          u32x2 o;
          o[0] = pack2(acc[m][n][0] * sc, acc[m][n][1] * sc);
          o[1] = pack2(acc[m][n][2] * sc, acc[m][n][3] * sc);
          *reinterpret_cast<u32x2*>(lds + (row0 + m * 16) * RS + (n * 16 + fq * 4) * 2) = o;
        }
    }
    __syncthreads();
    const int head0 = (n0 % D_) / DH_;
    const unsigned padw = (which == 2) ? 0x00003f80u : 0u;
    for (int it = threadIdx.x; it < 128 * 8; it += NTHR) {
      const int row = it >> 3, hd = it & 7;
      const unsigned* src = reinterpret_cast<const unsigned*>(lds + row * RS + hd * 36);
      u32x4 w0 = {src[0], src[1], src[2], src[3]};
      u32x4 w1 = {src[4], src[5], src[6], src[7]};
      u32x4 w2 = {src[8], padw, 0u, 0u};
      u32x4 w3 = {0u, 0u, 0u, 0u};
      u32x4* dst = reinterpret_cast<u32x4*>(qkv + ((size_t)(which * H_ + head0 + hd) * S_ + m0 + row) * HP);
      dst[0] = w0; dst[1] = w1; dst[2] = w2; dst[3] = w3;
    }
  }
};

constexpr int LDS_QKV = 4 * (128 * 64 * 2 + 64 * 144 * 2) + 2 * D_ * 4;
__global__ __launch_bounds__(256) void k_qkv(const u16* __restrict__ x, const u16* __restrict__ wqkv,
                                              const float2* __restrict__ st1, const float* __restrict__ mod,
                                              u16* __restrict__ qkv) {
  extern __shared__ __attribute__((aligned(16))) char lds[];
  const int b = blockIdx.x;
  const int xcd = b & 7, idx = b >> 3;
  const int bn = (idx % 3) * 4 + (xcd >> 1), bm = (idx / 3) * 2 + (xcd & 1);
  const int n0 = bn * 144;
  EpiQKV epi{qkv, bm * 128, n0};
  gemm_body<128, 144, 1, 4, 2, 1, 2, 64, 4, 1>(lds, x, D_, bm * 128, 0, D_ / 64, st1, mod + D_, mod, wqkv + n0, wqkv + n0, 3 * D_, epi);
}

__global__ __launch_bounds__(256, 2) void k_attn(const u16* __restrict__ Q, const u16* __restrict__ K,
                                                  const u16* __restrict__ V, u16* __restrict__ O) {
  __shared__ __attribute__((aligned(16))) char lds[3 * 16384];
  const int t = threadIdx.x, lane = t & 63, wid = t >> 6, r32 = lane & 31, h = lane >> 5;
  const int head = blockIdx.x >> 4, qb = blockIdx.x & 15;
  const int q0 = qb * 128 + wid * 32;
  const u16* Qh = Q + (size_t)head * S_ * HP;
  const u16* Kh = K + (size_t)head * S_ * HP;
  const u16* Vh = V + (size_t)head * S_ * HP;
  const bf16x8 qf0 = *reinterpret_cast<const bf16x8*>(Qh + (size_t)(q0 + r32) * HP + 8 * h);
  const bf16x8 qf1 = *reinterpret_cast<const bf16x8*>(Qh + (size_t)(q0 + r32) * HP + 16 + 8 * h);

  const int skey = t >> 2, sch = t & 3;
  const int kst_off = skey * 64 + ((sch ^ ((skey >> 2) & 3)) << 4);
  const int vst_off = 8192 + skey * 64 + sch * 16;
  u32x4 kreg0, kreg1, vreg0, vreg1;
  auto gload = [&](int st) {
    const u16* kp = Kh + (size_t)(st * 128 + skey) * HP + sch * 8;
    const u16* vp = Vh + (size_t)(st * 128 + skey) * HP + sch * 8;
    kreg0 = *reinterpret_cast<const u32x4*>(kp); kreg1 = *reinterpret_cast<const u32x4*>(kp + 64 * HP);
    vreg0 = *reinterpret_cast<const u32x4*>(vp); vreg1 = *reinterpret_cast<const u32x4*>(vp + 64 * HP);
  };
  auto swrite = [&](char* buf) {
    *reinterpret_cast<u32x4*>(buf + kst_off) = kreg0; *reinterpret_cast<u32x4*>(buf + kst_off + 4096) = kreg1;
    *reinterpret_cast<u32x4*>(buf + vst_off) = vreg0; *reinterpret_cast<u32x4*>(buf + vst_off + 4096) = vreg1;
  };

  f32x16 oacc, negm;
#pragma unroll
  for (int i = 0; i < 16; ++i) { oacc[i] = 0.f; negm[i] = 0.f; }
  constexpr float THR = 8.f;

  const int kr0 = r32 * 64 + (((0 + h) ^ ((r32 >> 2) & 3)) << 4);
  const int kr1 = r32 * 64 + (((2 + h) ^ ((r32 >> 2) & 3)) << 4);
  const int g = lane >> 4, q4 = (lane & 15) >> 2, p4 = lane & 3;
  const int vr = 8192 + (4 * (g >> 1) + q4) * 64 + (g & 1) * 32 + p4 * 8;

  constexpr int NS = S_ / 128;
  gload(0); swrite(lds);
  gload(1); swrite(lds + 16384);
  __syncthreads();
  auto qk = [&](const char* buf, int u) -> f32x16 {
    const bf16x8 ka = *reinterpret_cast<const bf16x8*>(buf + u * 2048 + kr0);
    const bf16x8 kb = *reinterpret_cast<const bf16x8*>(buf + u * 2048 + kr1);
    f32x16 s = __builtin_amdgcn_mfma_f32_32x32x16_bf16(ka, qf0, negm, 0, 0, 0);
    return __builtin_amdgcn_mfma_f32_32x32x16_bf16(kb, qf1, s, 0, 0, 0);
  };
  f32x16 sc = qk(lds, 0);
  int bcur = 0;
  for (int st = 0; st < NS; ++st) {
    const char* cur = lds + bcur * 16384;
    const int bn1 = bcur == 2 ? 0 : bcur + 1, bn2 = bn1 == 2 ? 0 : bn1 + 1;
    const char* nx1 = lds + bn1 * 16384;
    if (st + 2 < NS) gload(st + 2);
#pragma unroll
    for (int u = 0; u < 4; ++u) {
      f32x16 sn;
      if (u < 3) sn = qk(cur, u + 1);
      else sn = qk(nx1, 0);
      float mx = fmaxf(sc[0], sc[1]);
#pragma unroll
      for (int i = 2; i < 16; ++i) mx = fmaxf(mx, sc[i]);
      const bool first = (st == 0 && u == 0);
      if (first || !__all(mx <= THR)) {
        mx = fmaxf(mx, __shfl_xor(mx, 32));
        const float d = first ? mx : fmaxf(mx, 0.f);
        const float alpha = first ? 0.f : __builtin_amdgcn_exp2f(-d);
#pragma unroll
        for (int i = 0; i < 16; ++i) { oacc[i] *= alpha; negm[i] -= d; sc[i] -= d; sn[i] -= d; }
      }
      u32x4 pf0, pf1;
#pragma unroll
      for (int i = 0; i < 4; ++i) {
        pf0[i] = pack2(__builtin_amdgcn_exp2f(sc[2 * i]), __builtin_amdgcn_exp2f(sc[2 * i + 1]));
        pf1[i] = pack2(__builtin_amdgcn_exp2f(sc[8 + 2 * i]), __builtin_amdgcn_exp2f(sc[8 + 2 * i + 1]));
      }
      {
        s16x4 lo = tr_read(cur + vr + (32 * u) * 64);
        s16x4 hi = tr_read(cur + vr + (32 * u + 8) * 64);
        s16x4 lo2 = tr_read(cur + vr + (32 * u + 16) * 64);
        s16x4 hi2 = tr_read(cur + vr + (32 * u + 24) * 64);
        const bf16x8 vf0 = __builtin_shufflevector(lo, hi, 0, 1, 2, 3, 4, 5, 6, 7);
        const bf16x8 vf1 = __builtin_shufflevector(lo2, hi2, 0, 1, 2, 3, 4, 5, 6, 7);
        oacc = __builtin_amdgcn_mfma_f32_32x32x16_bf16(vf0, __builtin_bit_cast(bf16x8, pf0), oacc, 0, 0, 0);
        oacc = __builtin_amdgcn_mfma_f32_32x32x16_bf16(vf1, __builtin_bit_cast(bf16x8, pf1), oacc, 0, 0, 0);
      }
      sc = sn;
    }
    if (st + 2 < NS) swrite(lds + bn2 * 16384);
    __syncthreads();
    bcur = bn1;
  }
  const float l = __shfl(oacc[10], r32);
  const float inv = 1.f / l;
  unsigned* op = reinterpret_cast<unsigned*>(O + (size_t)(q0 + r32) * D_ + head * DH_);
  const unsigned w0 = pack2(oacc[0] * inv, oacc[1] * inv), w1 = pack2(oacc[2] * inv, oacc[3] * inv);
  const unsigned w2 = pack2(oacc[4] * inv, oacc[5] * inv), w3 = pack2(oacc[6] * inv, oacc[7] * inv);
  const unsigned w4 = pack2(oacc[8] * inv, oacc[9] * inv);
  op[2 * h] = w0; op[2 * h + 1] = w1; op[4 + 2 * h] = w2; op[5 + 2 * h] = w3;
  if (h == 0) op[8] = w4;
}

struct EpiProj {
  const u16* x; const float* ga; u16* x1; float2* st2; int m0, n0;
  template <int MT, int NT, int NTHR>
  DI void run(f32x4 (&acc)[MT][NT], char* lds, bool active, int row0, int wn, int fq) const {
    if (!active) return;
#pragma unroll
    for (int m = 0; m < MT; ++m) {
      const int row = m0 + row0 + m * 16;
      float v[NT][4];
      float s = 0.f;
#pragma unroll
      for (int n = 0; n < NT; ++n) {
        const int col = n0 + n * 16 + fq * 4;
        u32x2 xv = *reinterpret_cast<const u32x2*>(x + (size_t)row * D_ + col);
        f32x4 g = *reinterpret_cast<const f32x4*>(ga + col);
        u32x2 o;
        o[0] = pack2(bflo(xv[0]) + g[0] * acc[m][n][0], bfhi(xv[0]) + g[1] * acc[m][n][1]);
        o[1] = pack2(bflo(xv[1]) + g[2] * acc[m][n][2], bfhi(xv[1]) + g[3] * acc[m][n][3]);
        *reinterpret_cast<u32x2*>(x1 + (size_t)row * D_ + col) = o;
        v[n][0] = bflo(o[0]); v[n][1] = bfhi(o[0]); v[n][2] = bflo(o[1]); v[n][3] = bfhi(o[1]);
        s += (v[n][0] + v[n][1]) + (v[n][2] + v[n][3]);
      }
      s += __shfl_xor(s, 16); s += __shfl_xor(s, 32);
      const float lm = s * (1.f / (16 * NT));
      float q = 0.f;
#pragma unroll
      for (int n = 0; n < NT; ++n)
#pragma unroll
        for (int j = 0; j < 4; ++j) { float d = v[n][j] - lm; q += d * d; }
      q += __shfl_xor(q, 16); q += __shfl_xor(q, 32);
      if (fq == 0) st2[(size_t)row * 4 + n0 / 144] = make_float2(s, q);
    }
  }
};

constexpr int LDS_PROJ = 4 * (32 * 64 * 2 + 64 * 144 * 2);
__global__ __launch_bounds__(128) void k_proj(const u16* __restrict__ o, const u16* __restrict__ wo,
                                               const u16* __restrict__ x, const float* __restrict__ mod,
                                               u16* __restrict__ x1, float2* __restrict__ st2) {
  extern __shared__ __attribute__((aligned(16))) char lds[];
  const int b = blockIdx.x;
  const int bn = b & 3, bm = b >> 2;
  const int n0 = bn * 144;
  EpiProj epi{x, mod + 2 * D_, x1, st2, bm * 32, n0};
  gemm_body<32, 144, 1, 2, 1, 1, 2, 64, 4, 0>(lds, o, D_, bm * 32, 0, D_ / 64, nullptr, nullptr, nullptr, wo + n0, wo + n0, D_, epi);
}

__global__ __launch_bounds__(256) void k_ln2(const u16* __restrict__ x1, const float2* __restrict__ st2,
                                              const float* __restrict__ mod, u16* __restrict__ h2) {
  const int row = blockIdx.x * 4 + (threadIdx.x >> 6), lane = threadIdx.x & 63;
  const float2* p = st2 + (size_t)row * 4;
  float2 p0 = p[0], p1 = p[1], p2 = p[2], p3 = p[3];
  const float mean = (p0.x + p1.x + p2.x + p3.x) * (1.f / D_);
  float d0 = p0.x * (1.f / 144.f) - mean, d1 = p1.x * (1.f / 144.f) - mean, d2 = p2.x * (1.f / 144.f) - mean, d3 = p3.x * (1.f / 144.f) - mean;
  float m2 = p0.y + p1.y + p2.y + p3.y + 144.f * (d0 * d0 + d1 * d1 + d2 * d2 + d3 * d3);
  const float a = rsqrtf(m2 * (1.f / D_) + 1e-5f), b = -mean * a;
  const float* sb = mod + 3 * D_;
  const float* cb = mod + 4 * D_;
  for (int c = lane; c < 72; c += 64) {
    u32x4 r = *reinterpret_cast<const u32x4*>(x1 + (size_t)row * D_ + c * 8);
    f32x4 sc0 = *reinterpret_cast<const f32x4*>(cb + c * 8), sc1 = *reinterpret_cast<const f32x4*>(cb + c * 8 + 4);
    f32x4 sh0 = *reinterpret_cast<const f32x4*>(sb + c * 8), sh1 = *reinterpret_cast<const f32x4*>(sb + c * 8 + 4);
    u32x4 o;
    o[0] = pack2((bflo(r[0]) * a + b) * (sc0[0] + 1.f) + sh0[0], (bfhi(r[0]) * a + b) * (sc0[1] + 1.f) + sh0[1]);
    o[1] = pack2((bflo(r[1]) * a + b) * (sc0[2] + 1.f) + sh0[2], (bfhi(r[1]) * a + b) * (sc0[3] + 1.f) + sh0[3]);
    o[2] = pack2((bflo(r[2]) * a + b) * (sc1[0] + 1.f) + sh1[0], (bfhi(r[2]) * a + b) * (sc1[1] + 1.f) + sh1[1]);
    o[3] = pack2((bflo(r[3]) * a + b) * (sc1[2] + 1.f) + sh1[2], (bfhi(r[3]) * a + b) * (sc1[3] + 1.f) + sh1[3]);
    *reinterpret_cast<u32x4*>(h2 + (size_t)row * D_ + c * 8) = o;
  }
}

struct EpiGLU {
  u16* hh; int m0, n0;
  template <int MT, int NT, int NTHR>
  DI void run(f32x4 (&acc)[MT][NT], char* lds, bool active, int row0, int wn, int fq) const {
    constexpr int RS = 592;
#pragma unroll
    for (int m = 0; m < MT; ++m)
#pragma unroll
      for (int n = 0; n < NT; ++n) {
        u32x2 o;
        o[0] = pack2(acc[m][n][0], acc[m][n][1]);
        o[1] = pack2(acc[m][n][2], acc[m][n][3]);
        *reinterpret_cast<u32x2*>(lds + (row0 + m * 16) * RS + wn * 288 + (n * 16 + fq * 4) * 2) = o;
      }
    __syncthreads();
    for (int it = threadIdx.x; it < 256 * 18; it += NTHR) {
      const int row = it / 18, c = it % 18;
      const u32x4 g = *reinterpret_cast<const u32x4*>(lds + row * RS + c * 16);
      const u32x4 u = *reinterpret_cast<const u32x4*>(lds + row * RS + 288 + c * 16);
      u32x4 w;
#pragma unroll
      for (int j = 0; j < 4; ++j) w[j] = pack2(bflo(g[j]) * bflo(u[j]), bfhi(g[j]) * bfhi(u[j]));
      *reinterpret_cast<u32x4*>(hh + (size_t)(m0 + row) * F_ + n0 + c * 8) = w;
    }
  }
};

constexpr int LDS_GLU = 256 * 592;
__global__ __launch_bounds__(512) void k_glu(const u16* __restrict__ h2, const u16* __restrict__ wg,
                                              const u16* __restrict__ wu, u16* __restrict__ hh) {
  extern __shared__ __attribute__((aligned(16))) char lds[];
  const int b = blockIdx.x, xcd = b & 7, idx = b >> 3;
  const int bm = idx & 7, bn = xcd * 4 + (idx >> 3);
  const int n0 = bn * 144;
  EpiGLU epi{hh, bm * 256, n0};
  gemm_body<256, 288, 2, 4, 4, 2, 1, 32, 4, 0>(lds, h2, D_, bm * 256, 0, D_ / 32, nullptr, nullptr, nullptr, wg + n0, wu + n0, F_, epi);
}

struct EpiPart {
  float* part; int m0, n0;
  template <int MT, int NT, int NTHR>
  DI void run(f32x4 (&acc)[MT][NT], char* lds, bool active, int row0, int wn, int fq) const {
    constexpr int RS = 592;
    if (active) {
#pragma unroll
      for (int m = 0; m < MT; ++m)
#pragma unroll
        for (int n = 0; n < NT; ++n)
          *reinterpret_cast<f32x4*>(lds + (row0 + m * 16) * RS + (n * 16 + fq * 4) * 4) = acc[m][n];
    }
    __syncthreads();
    for (int it = threadIdx.x; it < 128 * 36; it += NTHR) {
      const int row = it / 36, c = it % 36;
      f32x4 w = *reinterpret_cast<const f32x4*>(lds + row * RS + c * 16);
      *reinterpret_cast<f32x4*>(part + (size_t)(m0 + row) * D_ + n0 + c * 4) = w;
    }
  }
};

constexpr int LDS_DOWN = 4 * 36 * 1024;
__global__ __launch_bounds__(512) void k_down(const u16* __restrict__ hh, const u16* __restrict__ wd,
                                               float* __restrict__ part) {
  extern __shared__ __attribute__((aligned(16))) char lds[];
  const int b = blockIdx.x, xcd = b & 7, idx = b >> 3;
  const int sk = xcd >> 1, bn = idx & 3, bm = (xcd & 1) * 8 + (idx >> 2);
  const int n0 = bn * 144;
  EpiPart epi{part + (size_t)sk * S_ * D_, bm * 128, n0};
  gemm_body<128, 144, 1, 4, 2, 1, 4, 128, 2, 0>(lds, hh, F_, bm * 128, sk * (F_ / 4), F_ / 4 / 128, nullptr, nullptr, nullptr, wd + n0, wd + n0, D_, epi);
}

__global__ __launch_bounds__(256) void k_final(const float* __restrict__ part, const u16* __restrict__ x1,
                                                const float* __restrict__ mod, u16* __restrict__ out) {
  const int i = (blockIdx.x * 256 + threadIdx.x) * 8;
  if (i >= S_ * D_) return;
  const int col = i % D_;
  const float* gb = mod + 5 * D_ + col;
  u32x4 xv = *reinterpret_cast<const u32x4*>(x1 + i);
  u32x4 o;
#pragma unroll
  for (int j = 0; j < 4; ++j) {
    float a = 0.f, b = 0.f;
#pragma unroll
    for (int s = 0; s < 4; ++s) {
      f32x2 p = *reinterpret_cast<const f32x2*>(part + (size_t)s * S_ * D_ + i + 2 * j);
      a += p[0]; b += p[1];
    }
    o[j] = pack2(bflo(xv[j]) + gb[2 * j] * a, bfhi(xv[j]) + gb[2 * j + 1] * b);
  }
  *reinterpret_cast<u32x4*>(out + i) = o;
}

extern "C" void kernel_launch(void* const* d_in, const int* in_sizes, int n_in,
                              void* d_out, int out_size, void* d_ws, size_t ws_size,
                              hipStream_t stream) {
  const u16* x = (const u16*)d_in[0];
  const u16* wqkv = (const u16*)d_in[1];
  const u16* wo = (const u16*)d_in[2];
  const u16* wg = (const u16*)d_in[4];
  const u16* wu = (const u16*)d_in[5];
  const u16* wd = (const u16*)d_in[6];
  const u16* c = (const u16*)d_in[7];
  const u16* wmod = (const u16*)d_in[8];
  char* ws = (char*)d_ws;
  float* mod = (float*)(ws + WS_MOD);
  float2* st1 = (float2*)(ws + WS_ST1);
  float2* st2 = (float2*)(ws + WS_ST2);
  u16* Q = (u16*)(ws + WS_Q);
  u16* K = (u16*)(ws + WS_K);
  u16* V = (u16*)(ws + WS_V);
  u16* O = (u16*)(ws + WS_O);
  u16* x1 = (u16*)(ws + WS_X1);
  u16* h2 = (u16*)(ws + WS_H2);
  u16* hh = (u16*)(ws + WS_HH);
  float* part = (float*)(ws + WS_PART);

  (void)hipFuncSetAttribute((const void*)k_qkv, hipFuncAttributeMaxDynamicSharedMemorySize, LDS_QKV);
  (void)hipFuncSetAttribute((const void*)k_proj, hipFuncAttributeMaxDynamicSharedMemorySize, LDS_PROJ);
  (void)hipFuncSetAttribute((const void*)k_glu, hipFuncAttributeMaxDynamicSharedMemorySize, LDS_GLU);
  (void)hipFuncSetAttribute((const void*)k_down, hipFuncAttributeMaxDynamicSharedMemorySize, LDS_DOWN);

  k_prep<<<108 + 512, 256, 0, stream>>>(c, wmod, x, mod, st1);
  k_qkv<<<192, 256, LDS_QKV, stream>>>(x, wqkv, st1, mod, Q);
  k_attn<<<32 * 16, 256, 0, stream>>>(Q, K, V, O);
  k_proj<<<256, 128, LDS_PROJ, stream>>>(O, wo, x, mod, x1, st2);
  k_ln2<<<512, 256, 0, stream>>>(x1, st2, mod, h2);
  k_glu<<<256, 512, LDS_GLU, stream>>>(h2, wg, wu, hh);
  k_down<<<256, 512, LDS_DOWN, stream>>>(hh, wd, part);
  k_final<<<(S_ * D_ / 8 + 255) / 256, 256, 0, stream>>>(part, x1, mod, (u16*)d_out);
}
```

```cpp
#include <hip/hip_runtime.h>
#include <hip/hip_bf16.h>
#include <stdint.h>
#include <type_traits>

typedef __attribute__((ext_vector_type(8))) short bf16x8;
typedef __attribute__((ext_vector_type(4))) short s16x4;
typedef __attribute__((ext_vector_type(4))) float f32x4;
typedef __attribute__((ext_vector_type(2))) float f32x2;
typedef __attribute__((ext_vector_type(16))) float f32x16;
typedef __attribute__((ext_vector_type(4))) unsigned u32x4;
typedef __attribute__((ext_vector_type(2))) unsigned u32x2;
typedef __attribute__((ext_vector_type(2))) __bf16 bf16x2_t;
typedef unsigned short u16;

#define DI __device__ __forceinline__

constexpr int S_ = 2048, D_ = 576, H_ = 32, DH_ = 18, F_ = 4608;
constexpr int HP = 32;
constexpr float QSCALE = 0.23570226039551584f * 1.4426950408889634f;

DI float bf2f(u16 v) { return __uint_as_float(((unsigned)v) << 16); }
DI float bflo(unsigned v) { return __uint_as_float(v << 16); }
DI float bfhi(unsigned v) { return __uint_as_float(v & 0xffff0000u); }
DI unsigned pack2(float a, float b) {
  f32x2 v = {a, b};
  bf16x2_t r = __builtin_convertvector(v, bf16x2_t);
  return __builtin_bit_cast(unsigned, r);
}
DI u16 f2bf(float a) { return (u16)(pack2(a, 0.f) & 0xffffu); }
DI float rbf(float a) { return bflo(pack2(a, 0.f) & 0xffffu); }

typedef __attribute__((address_space(3))) s16x4* lds_s16x4_ptr;
DI s16x4 tr_read(const char* p) {
  return __builtin_amdgcn_ds_read_tr16_b64_v4i16((lds_s16x4_ptr)(p));
}

constexpr size_t WS_MOD = 0;
constexpr size_t WS_ST1 = 16384;
constexpr size_t WS_ST2 = WS_ST1 + 2048 * 8;
constexpr size_t WS_Q = 1 << 20;
constexpr size_t WS_K = WS_Q + (size_t)H_ * S_ * HP * 2;
constexpr size_t WS_V = WS_K + (size_t)H_ * S_ * HP * 2;
constexpr size_t WS_O = WS_V + (size_t)H_ * S_ * HP * 2;
constexpr size_t WS_X1 = WS_O + (size_t)S_ * D_ * 2;
constexpr size_t WS_H2 = WS_X1 + (size_t)S_ * D_ * 2;
constexpr size_t WS_HH = WS_H2 + (size_t)S_ * D_ * 2;
constexpr size_t WS_PART = WS_HH + (size_t)S_ * F_ * 2;

__global__ __launch_bounds__(256) void k_prep(const u16* __restrict__ c, const u16* __restrict__ wmod,
                                               const u16* __restrict__ x, float* __restrict__ mod,
                                               float2* __restrict__ st1) {
  __shared__ float red[64][33];
  const int t = threadIdx.x;
  if (blockIdx.x < 108) {
    const int n0 = blockIdx.x * 32 + (t & 3) * 8;
    const int ks = t >> 2;
    float acc[8];
#pragma unroll
    for (int j = 0; j < 8; ++j) acc[j] = 0.f;
#pragma unroll
    for (int r = 0; r < 9; ++r) {
      const int k = ks * 9 + r;
      float cv = bf2f(c[k]);
      float sv = rbf(cv / (1.f + __expf(-cv)));
      u32x4 w = *reinterpret_cast<const u32x4*>(wmod + (size_t)k * 3456 + n0);
#pragma unroll
      for (int j = 0; j < 4; ++j) {
        acc[2 * j] += sv * bflo(w[j]);
        acc[2 * j + 1] += sv * bfhi(w[j]);
      }
    }
#pragma unroll
    for (int j = 0; j < 8; ++j) red[ks][(t & 3) * 8 + j] = acc[j];
    __syncthreads();
    if (t < 32) {
      float s = 0.f;
#pragma unroll 8
      for (int i = 0; i < 64; ++i) s += red[i][t];
      mod[blockIdx.x * 32 + t] = rbf(s);
    }
  } else {
    const int lane = t & 63, w = t >> 6;
    for (int row = (blockIdx.x - 108) * 4 + w; row < S_; row += (gridDim.x - 108) * 4) {
      const u16* xr = x + (size_t)row * D_;
      u32x4 a = *reinterpret_cast<const u32x4*>(xr + lane * 8);
      u32x4 b = {0, 0, 0, 0};
      if (lane < 8) b = *reinterpret_cast<const u32x4*>(xr + 512 + lane * 8);
      float v[16];
#pragma unroll
      for (int j = 0; j < 4; ++j) {
        v[2 * j] = bflo(a[j]); v[2 * j + 1] = bfhi(a[j]);
        v[8 + 2 * j] = bflo(b[j]); v[8 + 2 * j + 1] = bfhi(b[j]);
      }
      float s = 0.f;
#pragma unroll
      for (int j = 0; j < 16; ++j) s += v[j];
#pragma unroll
      for (int o = 32; o >= 1; o >>= 1) s += __shfl_xor(s, o);
      const float mean = s * (1.f / D_);
      float q = 0.f;
#pragma unroll
      for (int j = 0; j < 8; ++j) { float d = v[j] - mean; q += d * d; }
      if (lane < 8) {
#pragma unroll
        for (int j = 8; j < 16; ++j) { float d = v[j] - mean; q += d * d; }
      }
#pragma unroll
      for (int o = 32; o >= 1; o >>= 1) q += __shfl_xor(q, o);
      if (lane == 0) st1[row] = make_float2(mean, rsqrtf(q * (1.f / D_) + 1e-5f));
    }
  }
}

#define WAIT_LGKM0() asm volatile("s_waitcnt lgkmcnt(0)" ::: "memory")
#define STAMP(i)
#define RAW_BAR() __builtin_amdgcn_s_barrier()
template <int N> DI void wait_vm() { asm volatile("s_waitcnt vmcnt(%0)" ::"n"(N) : "memory"); }
template <int OFF> DI void lds_tr(u32x2& d, unsigned a) { asm volatile("ds_read_b64_tr_b16 %0, %1 offset:%2" : "=v"(d) : "v"(a), "n"(OFF)); }
template <int OFF> DI void lds_128(u32x4& d, unsigned a) { asm volatile("ds_read_b128 %0, %1 offset:%2" : "=v"(d) : "v"(a), "n"(OFF)); }
DI void pin4(u32x4& a) { asm volatile("" : "+v"(a)); }
template <int N> DI void wait_l2(u32x2& a, u32x2& b) { asm volatile("s_waitcnt lgkmcnt(%2)" : "+v"(a), "+v"(b) : "n"(N)); }
template <int N> DI void wait_l4(u32x2& a, u32x2& b, u32x4& c, u32x4& d) { asm volatile("s_waitcnt lgkmcnt(%4)" : "+v"(a), "+v"(b), "+v"(c), "+v"(d) : "n"(N)); }

template <int BK> DI int a_swz(int row) { return BK >= 64 ? ((row >> 1) & 7) : ((-(row >> 2)) & 3); }

template <int I, int N, class F> DI void static_for(F&& f) {
  if constexpr (I < N) { f(std::integral_constant<int, I>{}); static_for<I + 1, N>(f); }
}

template <int BM, int BN, int NIMG, int MT, int WM, int WN, int BK, int NST, int LNMODE  , class Epi>
DI void gemm_body(char* lds, const u16* __restrict__ A, int lda, int m0, int kbeg, int nk,
                  const float2* __restrict__ stats, const float* __restrict__ mscale, const float* __restrict__ mshift,
                  const u16* __restrict__ B0, const u16* __restrict__ B1, int ldb, Epi& epi) {
  constexpr int NW = WM * WN, NTHR = 64 * NW;
  constexpr int NT = BN / 16, NTW0 = (NT + WN - 1) / WN, NTWL = NT - (WN - 1) * NTW0, BNI = BN / NIMG, NTI = NT / NIMG;
  constexpr int ABYTES = BM * BK * 2, BBYTES = BK * BN * 2, STAGE = ABYTES + BBYTES;
  constexpr int AINS = ABYTES / 1024, BINS = BBYTES / 1024;
  constexpr int BINS_I = BINS / NIMG;
  constexpr int TINS = AINS + BINS;
  constexpr int G = (TINS + NW - 1) / NW;
  constexpr int SUB = BK / 32;
  constexpr int CPR = BK >= 64 ? 8 : 4;
  constexpr int ARB = CPR * 16;
  static_assert(BBYTES % (1024 * NIMG) == 0 && ABYTES % 1024 == 0 && BM == WM * MT * 16, "tile");
  static_assert(NIMG == 1 || NTW0 == NTI, "img");
  const int t = threadIdx.x, lane = t & 63;
  const int wid = __builtin_amdgcn_readfirstlane(t >> 6);
  const int wr = wid % WM, wn = wid / WM, fr = lane & 15, fq = lane >> 4;
  float* lnp = reinterpret_cast<float*>(lds + NST * STAGE);

  STAMP(0);
  unsigned goff[G];
#pragma unroll
  for (int ii = 0; ii < G; ++ii) {
    int i = wid + ii * NW;
    if (i >= TINS) i -= TINS;
    if (i < AINS) {
      const int j = i * 64 + lane;
      const int si = j / (BM * CPR), row = (j / CPR) % BM, pos = j % CPR;
      goff[ii] = (unsigned)((row * lda + si * 64 + (pos ^ a_swz<BK>(row)) * 8) * 2);
    } else {
      const int j = ((i - AINS) % BINS_I) * 64 + lane;
      const int kb = j / BNI, rem = j % BNI;
      const int sub = rem >> 4, r = (rem >> 1) & 7, hf = rem & 1;
      goff[ii] = (unsigned)(((kb * 8 + (r ^ ((kb & 1) << 2))) * ldb + sub * 16 + hf * 8) * 2);
    }
  }
  const char* Abase = (const char*)(A + (size_t)m0 * lda + kbeg);
  const char* B0base = (const char*)(B0 + (size_t)kbeg * ldb);
  const char* B1base = (const char*)(B1 + (size_t)kbeg * ldb);

  auto issue_one = [&](int kt, int ii) {
    char* buf = lds + (kt % NST) * STAGE;
    int i = wid + ii * NW;
    if (i >= TINS) i -= TINS;
    const char* base = (i < AINS) ? (Abase + kt * BK * 2)
                                  : (((NIMG == 2 && i - AINS >= BINS_I) ? B1base : B0base) + (size_t)kt * BK * ldb * 2);
    __builtin_amdgcn_global_load_lds((const unsigned*)(base + goff[ii]),
                                     (__attribute__((address_space(3))) unsigned*)(buf + (i * 64 + lane) * 16), 16, 0, 0);
  };

#pragma unroll
  for (int st = 0; st < NST - 1; ++st)
    if (st < nk) {
#pragma unroll
      for (int ii = 0; ii < G; ++ii) issue_one(st, ii);
    }

  float ra[MT], rb[MT];
  if constexpr (LNMODE != 0) {
    for (int i = t; i < D_; i += NTHR) { lnp[i] = mscale[i] + 1.f; lnp[D_ + i] = mshift[i]; }
#pragma unroll
    for (int m = 0; m < MT; ++m) {
      float2 s = stats[m0 + (wr * MT + m) * 16 + fr];
      ra[m] = s.y; rb[m] = -s.x * s.y;
    }
    wait_vm<0>();
    WAIT_LGKM0();
  }

  f32x4 acc[MT][NTW0];
#pragma unroll
  for (int m = 0; m < MT; ++m)
#pragma unroll
    for (int n = 0; n < NTW0; ++n) acc[m][n] = f32x4{0.f, 0.f, 0.f, 0.f};

  const unsigned lbase = (unsigned)(size_t)lds;
  const int q4 = fr >> 2, p4 = lane & 3, sw = (fq & 1) << 2;
  const int arow = wr * MT * 16 + fr;
  const unsigned aoffb = arow * ARB;
  const int tile0 = wn * NTW0;
  const unsigned bimg = (unsigned)((tile0 / NTI) * (BBYTES / NIMG) + (tile0 % NTI) * 256);
  const unsigned boff_lo = ABYTES + bimg + fq * (BNI * 16) + ((q4 ^ sw) << 5) + p4 * 8;
  const unsigned boff_hi = ABYTES + bimg + fq * (BNI * 16) + (((q4 + 4) ^ sw) << 5) + p4 * 8;

  auto mainloop = [&](auto NTWC) {
    constexpr int NTW = decltype(NTWC)::value;
    constexpr int PF = 3;
    constexpr int T = SUB * NTW;
    for (int kt = 0; kt < nk; ++kt) {
      STAMP(2 + 2 * kt);
      if (kt + NST - 2 < nk) wait_vm<G * (NST - 2)>(); else wait_vm<0>();
      RAW_BAR();
      STAMP(3 + 2 * kt);
      const bool more = kt + NST - 1 < nk;
      const unsigned sb = lbase + (kt % NST) * STAGE;
      const unsigned vb_lo = sb + boff_lo, vb_hi = sb + boff_hi;
      u32x2 blo[PF], bhi[PF];
      u32x4 af[2][MT];
      auto rda = [&](auto S2) {
        constexpr int s2 = decltype(S2)::value;
        const int ch = (s2 & (CPR / 4 - 1)) * 4 + fq;
        const unsigned base = sb + (s2 / (CPR / 4)) * (BM * ARB) + aoffb;
        lds_128<0>(af[s2 & 1][0], base + ((ch ^ a_swz<BK>(arow)) << 4));
        if constexpr (MT > 1) lds_128<16 * ARB>(af[s2 & 1][MT > 1 ? 1 : 0], base + ((ch ^ a_swz<BK>(arow + 16)) << 4));
        if constexpr (MT > 2) {
          lds_128<32 * ARB>(af[s2 & 1][MT > 2 ? 2 : 0], base + ((ch ^ a_swz<BK>(arow + 32)) << 4));
          lds_128<48 * ARB>(af[s2 & 1][MT > 2 ? 3 : 0], base + ((ch ^ a_swz<BK>(arow + 48)) << 4));
        }
      };
      auto rdb = [&](auto IDX) {
        constexpr int idx = decltype(IDX)::value;
        constexpr int s2 = idx / NTW, n = idx % NTW;
        constexpr int off = s2 * 4 * (BNI * 16) + n * 256;
        lds_tr<off>(blo[idx % PF], vb_lo);
        lds_tr<off>(bhi[idx % PF], vb_hi);
      };
      rda(std::integral_constant<int, 0>{});
      static_for<0, PF - 1>([&](auto I) { rdb(I); });
      static_for<0, T>([&](auto IDX) {
        constexpr int idx = decltype(IDX)::value;
        constexpr int s2 = idx / NTW, n = idx % NTW;
        if constexpr (n == NTW - PF && s2 + 1 < SUB) rda(std::integral_constant<int, s2 + 1>{});
        if constexpr (idx + PF - 1 < T) rdb(std::integral_constant<int, idx + PF - 1>{});
        constexpr int nb = ((idx + PF - 1 < T) ? (idx + PF - 1) : (T - 1)) - idx;
        constexpr int sA = (idx + PF) / NTW - 1;
        constexpr int idxA = sA * NTW + NTW - PF;
        constexpr bool a_after = (sA >= 0) && (sA + 1 < SUB) && (idxA <= idx) && (idx <= idxA + PF - 2);
        constexpr int cnt = 2 * nb + (a_after ? MT : 0);
        if constexpr (n == 0) {
          asm volatile("s_waitcnt lgkmcnt(%0)" ::"n"(cnt));
          pin4(af[s2 & 1][0]);
          if constexpr (MT > 1) pin4(af[s2 & 1][MT > 1 ? 1 : 0]);
          if constexpr (MT > 2) { pin4(af[s2 & 1][MT - 2]); pin4(af[s2 & 1][MT - 1]); }
          asm volatile("" : "+v"(blo[idx % PF]), "+v"(bhi[idx % PF]));
          if constexpr (LNMODE != 0) {
            const int kc = kbeg + kt * BK + s2 * 32 + fq * 8;
            f32x4 sc0 = *reinterpret_cast<const f32x4*>(lnp + kc), sc1 = *reinterpret_cast<const f32x4*>(lnp + kc + 4);
            f32x4 sh0 = *reinterpret_cast<const f32x4*>(lnp + D_ + kc), sh1 = *reinterpret_cast<const f32x4*>(lnp + D_ + kc + 4);
#pragma unroll
            for (int m = 0; m < MT; ++m) {
              u32x4 r = af[s2 & 1][m], o;
              const float a = ra[m], b = rb[m];
              o[0] = pack2((bflo(r[0]) * a + b) * sc0[0] + sh0[0], (bfhi(r[0]) * a + b) * sc0[1] + sh0[1]);
              o[1] = pack2((bflo(r[1]) * a + b) * sc0[2] + sh0[2], (bfhi(r[1]) * a + b) * sc0[3] + sh0[3]);
              o[2] = pack2((bflo(r[2]) * a + b) * sc1[0] + sh1[0], (bfhi(r[2]) * a + b) * sc1[1] + sh1[1]);
              o[3] = pack2((bflo(r[3]) * a + b) * sc1[2] + sh1[2], (bfhi(r[3]) * a + b) * sc1[3] + sh1[3]);
              af[s2 & 1][m] = o;
            }
          }
        } else {
          wait_l2<cnt>(blo[idx % PF], bhi[idx % PF]);
        }
        u32x4 bw = {blo[idx % PF][0], blo[idx % PF][1], bhi[idx % PF][0], bhi[idx % PF][1]};
        const bf16x8 bf = __builtin_bit_cast(bf16x8, bw);
#pragma unroll
        for (int m = 0; m < MT; ++m)
          acc[m][n] = __builtin_amdgcn_mfma_f32_16x16x32_bf16(bf, __builtin_bit_cast(bf16x8, af[s2 & 1][m]), acc[m][n], 0, 0, 0);
        static_for<0, G>([&](auto II) {
          constexpr int ii = decltype(II)::value;
          if constexpr (idx == (ii * T) / G) { if (more) issue_one(kt + NST - 1, ii); }
        });
      });
    }
  };
  if constexpr (NTWL != NTW0) {
    if (wn == WN - 1) mainloop(std::integral_constant<int, NTWL>{});
    else mainloop(std::integral_constant<int, NTW0>{});
  } else {
    mainloop(std::integral_constant<int, NTW0>{});
  }
  STAMP(50);
  __syncthreads();
  STAMP(51);
  const int ntw = (wn == WN - 1) ? NTWL : NTW0;
  epi.template run<MT, NTW0, NTHR>(acc, lds, wr * MT * 16 + fr, tile0, ntw, fq);
  STAMP(52);
}

struct EpiQKV {
  u16* qkv;
  int m0, n0;
  template <int MT, int NTW, int NTHR>
  DI void run(f32x4 (&acc)[MT][NTW], char* lds, int row0, int tile0, int ntw, int fq) const {
    constexpr int RS = 296;
    const int which = n0 / D_;
    const float sc = which == 0 ? QSCALE : 1.f;
#pragma unroll
    for (int m = 0; m < MT; ++m)
#pragma unroll
      for (int n = 0; n < NTW; ++n) {
        if (n < ntw) {
          u32x2 o;
          o[0] = pack2(acc[m][n][0] * sc, acc[m][n][1] * sc);
          o[1] = pack2(acc[m][n][2] * sc, acc[m][n][3] * sc);
          *reinterpret_cast<u32x2*>(lds + (row0 + m * 16) * RS + ((tile0 + n) * 16 + fq * 4) * 2) = o;
        }
      }
    __syncthreads();
    const int head0 = (n0 % D_) / DH_;
    const unsigned padw = (which == 2) ? 0x00003f80u : 0u;
    for (int it = threadIdx.x; it < 8 * 128 * 4; it += NTHR) {
      const int ch = it & 3, row = (it >> 2) & 127, hd = it >> 9;
      const unsigned* src = reinterpret_cast<const unsigned*>(lds + row * RS + hd * 36 + ch * 16);
      u32x4 w = {0u, 0u, 0u, 0u};
      if (ch < 2) w = u32x4{src[0], src[1], src[2], src[3]};
      else if (ch == 2) w = u32x4{src[0], padw, 0u, 0u};
      *reinterpret_cast<u32x4*>(qkv + ((size_t)(which * H_ + head0 + hd) * S_ + m0 + row) * HP + ch * 8) = w;
    }
  }
};

constexpr int LDS_QKV = 4 * (128 * 64 * 2 + 64 * 144 * 2) + 2 * D_ * 4;
__global__ __launch_bounds__(512) void k_qkv(const u16* __restrict__ x, const u16* __restrict__ wqkv,
                                              const float2* __restrict__ st1, const float* __restrict__ mod,
                                              u16* __restrict__ qkv) {
  extern __shared__ __attribute__((aligned(16))) char lds[];
  const int b = blockIdx.x;
  const int xcd = b & 7, idx = b >> 3;
  const int bn = (idx % 3) * 4 + (xcd >> 1), bm = (idx / 3) * 2 + (xcd & 1);
  const int n0 = bn * 144;
  EpiQKV epi{qkv, bm * 128, n0};
  gemm_body<128, 144, 1, 2, 4, 2, 64, 4, 1>(lds, x, D_, bm * 128, 0, D_ / 64, st1, mod + D_, mod, wqkv + n0, wqkv + n0, 3 * D_, epi);
}

__global__ __launch_bounds__(256, 2) void k_attn(const u16* __restrict__ Q, const u16* __restrict__ K,
                                                  const u16* __restrict__ V, u16* __restrict__ O) {
  __shared__ __attribute__((aligned(16))) char lds[3 * 16384];
  const int t = threadIdx.x, lane = t & 63, wid = t >> 6, r32 = lane & 31, h = lane >> 5;
  const int head = blockIdx.x >> 4, qb = blockIdx.x & 15;
  const int q0 = qb * 128 + wid * 32;
  const u16* Qh = Q + (size_t)head * S_ * HP;
  const u16* Kh = K + (size_t)head * S_ * HP;
  const u16* Vh = V + (size_t)head * S_ * HP;
  const bf16x8 qf0 = *reinterpret_cast<const bf16x8*>(Qh + (size_t)(q0 + r32) * HP + 8 * h);
  const bf16x8 qf1 = *reinterpret_cast<const bf16x8*>(Qh + (size_t)(q0 + r32) * HP + 16 + 8 * h);

  const int skey = t >> 2, sch = t & 3;
  const int kst_off = skey * 64 + ((sch ^ ((skey >> 2) & 3)) << 4);
  const int vst_off = 8192 + skey * 64 + sch * 16;
  u32x4 kreg0, kreg1, vreg0, vreg1;
  auto gload = [&](int st) {
    const u16* kp = Kh + (size_t)(st * 128 + skey) * HP + sch * 8;
    const u16* vp = Vh + (size_t)(st * 128 + skey) * HP + sch * 8;
    kreg0 = *reinterpret_cast<const u32x4*>(kp); kreg1 = *reinterpret_cast<const u32x4*>(kp + 64 * HP);
    vreg0 = *reinterpret_cast<const u32x4*>(vp); vreg1 = *reinterpret_cast<const u32x4*>(vp + 64 * HP);
  };
  auto swrite = [&](char* buf) {
    *reinterpret_cast<u32x4*>(buf + kst_off) = kreg0; *reinterpret_cast<u32x4*>(buf + kst_off + 4096) = kreg1;
    *reinterpret_cast<u32x4*>(buf + vst_off) = vreg0; *reinterpret_cast<u32x4*>(buf + vst_off + 4096) = vreg1;
  };

  f32x16 oacc, negm;
#pragma unroll
  for (int i = 0; i < 16; ++i) { oacc[i] = 0.f; negm[i] = 0.f; }
  constexpr float THR = 8.f;

  const int kr0 = r32 * 64 + (((0 + h) ^ ((r32 >> 2) & 3)) << 4);
  const int kr1 = r32 * 64 + (((2 + h) ^ ((r32 >> 2) & 3)) << 4);
  const int g = lane >> 4, q4 = (lane & 15) >> 2, p4 = lane & 3;
  const int vr = 8192 + (4 * (g >> 1) + q4) * 64 + (g & 1) * 32 + p4 * 8;

  constexpr int NS = S_ / 128;
  gload(0); swrite(lds);
  gload(1); swrite(lds + 16384);
  __syncthreads();
  auto qk = [&](const char* buf, int u) -> f32x16 {
    const bf16x8 ka = *reinterpret_cast<const bf16x8*>(buf + u * 2048 + kr0);
    const bf16x8 kb = *reinterpret_cast<const bf16x8*>(buf + u * 2048 + kr1);
    f32x16 s = __builtin_amdgcn_mfma_f32_32x32x16_bf16(ka, qf0, negm, 0, 0, 0);
    return __builtin_amdgcn_mfma_f32_32x32x16_bf16(kb, qf1, s, 0, 0, 0);
  };
  f32x16 sc = qk(lds, 0);
  int bcur = 0;
  for (int st = 0; st < NS; ++st) {
    const char* cur = lds + bcur * 16384;
    const int bn1 = bcur == 2 ? 0 : bcur + 1, bn2 = bn1 == 2 ? 0 : bn1 + 1;
    const char* nx1 = lds + bn1 * 16384;
    if (st + 2 < NS) gload(st + 2);
#pragma unroll
    for (int u = 0; u < 4; ++u) {
      f32x16 sn;
      if (u < 3) sn = qk(cur, u + 1);
      else sn = qk(nx1, 0);
      float mx = fmaxf(sc[0], sc[1]);
#pragma unroll
      for (int i = 2; i < 16; ++i) mx = fmaxf(mx, sc[i]);
      const bool first = (st == 0 && u == 0);
      if (first || !__all(mx <= THR)) {
        mx = fmaxf(mx, __shfl_xor(mx, 32));
        const float d = first ? mx : fmaxf(mx, 0.f);
        const float alpha = first ? 0.f : __builtin_amdgcn_exp2f(-d);
#pragma unroll
        for (int i = 0; i < 16; ++i) { oacc[i] *= alpha; negm[i] -= d; sc[i] -= d; sn[i] -= d; }
      }
      u32x4 pf0, pf1;
#pragma unroll
      for (int i = 0; i < 4; ++i) {
        pf0[i] = pack2(__builtin_amdgcn_exp2f(sc[2 * i]), __builtin_amdgcn_exp2f(sc[2 * i + 1]));
        pf1[i] = pack2(__builtin_amdgcn_exp2f(sc[8 + 2 * i]), __builtin_amdgcn_exp2f(sc[8 + 2 * i + 1]));
      }
      {
        s16x4 lo = tr_read(cur + vr + (32 * u) * 64);
        s16x4 hi = tr_read(cur + vr + (32 * u + 8) * 64);
        s16x4 lo2 = tr_read(cur + vr + (32 * u + 16) * 64);
        s16x4 hi2 = tr_read(cur + vr + (32 * u + 24) * 64);
        const bf16x8 vf0 = __builtin_shufflevector(lo, hi, 0, 1, 2, 3, 4, 5, 6, 7);
        const bf16x8 vf1 = __builtin_shufflevector(lo2, hi2, 0, 1, 2, 3, 4, 5, 6, 7);
        oacc = __builtin_amdgcn_mfma_f32_32x32x16_bf16(vf0, __builtin_bit_cast(bf16x8, pf0), oacc, 0, 0, 0);
        oacc = __builtin_amdgcn_mfma_f32_32x32x16_bf16(vf1, __builtin_bit_cast(bf16x8, pf1), oacc, 0, 0, 0);
      }
      sc = sn;
    }
    if (st + 2 < NS) swrite(lds + bn2 * 16384);
    __syncthreads();
    bcur = bn1;
  }
  const float l = __shfl(oacc[10], r32);
  const float inv = 1.f / l;
  unsigned* op = reinterpret_cast<unsigned*>(O + (size_t)(q0 + r32) * D_ + head * DH_);
  const unsigned w0 = pack2(oacc[0] * inv, oacc[1] * inv), w1 = pack2(oacc[2] * inv, oacc[3] * inv);
  const unsigned w2 = pack2(oacc[4] * inv, oacc[5] * inv), w3 = pack2(oacc[6] * inv, oacc[7] * inv);
  const unsigned w4 = pack2(oacc[8] * inv, oacc[9] * inv);
  op[2 * h] = w0; op[2 * h + 1] = w1; op[4 + 2 * h] = w2; op[5 + 2 * h] = w3;
  if (h == 0) op[8] = w4;
}

struct EpiProj {
  const u16* x; const float* mod; u16* x1; u16* h2; int m0;
  template <int MT, int NTW, int NTHR>
  DI void run(f32x4 (&acc)[MT][NTW], char* lds, int row0, int tile0, int ntw, int fq) const {
    constexpr int NWV = NTHR / 64;
    float* red = reinterpret_cast<float*>(lds);
    const int wv = threadIdx.x >> 6;
    const float* ga = mod + 2 * D_;
    float s[MT];
#pragma unroll
    for (int m = 0; m < MT; ++m) {
      const int row = m0 + row0 + m * 16;
      s[m] = 0.f;
#pragma unroll
      for (int n = 0; n < NTW; ++n) {
        const int col = (tile0 + n) * 16 + fq * 4;
        u32x2 xv = *reinterpret_cast<const u32x2*>(x + (size_t)row * D_ + col);
        f32x4 g = *reinterpret_cast<const f32x4*>(ga + col);
        u32x2 o;
        o[0] = pack2(bflo(xv[0]) + g[0] * acc[m][n][0], bfhi(xv[0]) + g[1] * acc[m][n][1]);
        o[1] = pack2(bflo(xv[1]) + g[2] * acc[m][n][2], bfhi(xv[1]) + g[3] * acc[m][n][3]);
        *reinterpret_cast<u32x2*>(x1 + (size_t)row * D_ + col) = o;
        acc[m][n] = f32x4{bflo(o[0]), bfhi(o[0]), bflo(o[1]), bfhi(o[1])};
        s[m] += (acc[m][n][0] + acc[m][n][1]) + (acc[m][n][2] + acc[m][n][3]);
      }
      s[m] += __shfl_xor(s[m], 16); s[m] += __shfl_xor(s[m], 32);
      if (fq == 0) red[wv * 32 + row0 + m * 16] = s[m];
    }
    __syncthreads();
    float mean[MT], q[MT];
#pragma unroll
    for (int m = 0; m < MT; ++m) {
      float t = 0.f;
#pragma unroll
      for (int w = 0; w < NWV; ++w) t += red[w * 32 + row0 + m * 16];
      mean[m] = t * (1.f / D_);
      q[m] = 0.f;
#pragma unroll
      for (int n = 0; n < NTW; ++n)
#pragma unroll
        for (int j = 0; j < 4; ++j) { float d = acc[m][n][j] - mean[m]; q[m] += d * d; }
      q[m] += __shfl_xor(q[m], 16); q[m] += __shfl_xor(q[m], 32);
      if (fq == 0) red[NWV * 32 + wv * 32 + row0 + m * 16] = q[m];
    }
    __syncthreads();
    const float* sb = mod + 3 * D_;
    const float* cb = mod + 4 * D_;
#pragma unroll
    for (int m = 0; m < MT; ++m) {
      float t = 0.f;
#pragma unroll
      for (int w = 0; w < NWV; ++w) t += red[NWV * 32 + w * 32 + row0 + m * 16];
      const float a = rsqrtf(t * (1.f / D_) + 1e-5f), b = -mean[m] * a;
      const int row = m0 + row0 + m * 16;
#pragma unroll
      for (int n = 0; n < NTW; ++n) {
        const int col = (tile0 + n) * 16 + fq * 4;
        f32x4 sc = *reinterpret_cast<const f32x4*>(cb + col), sh = *reinterpret_cast<const f32x4*>(sb + col);
        u32x2 o;
        o[0] = pack2((acc[m][n][0] * a + b) * (sc[0] + 1.f) + sh[0], (acc[m][n][1] * a + b) * (sc[1] + 1.f) + sh[1]);
        o[1] = pack2((acc[m][n][2] * a + b) * (sc[2] + 1.f) + sh[2], (acc[m][n][3] * a + b) * (sc[3] + 1.f) + sh[3]);
        *reinterpret_cast<u32x2*>(h2 + (size_t)row * D_ + col) = o;
      }
    }
  }
};

constexpr int LDS_PROJ = 4 * (32 * 32 * 2 + 32 * 576 * 2);
__global__ __launch_bounds__(384) void k_proj(const u16* __restrict__ o, const u16* __restrict__ wo,
                                               const u16* __restrict__ x, const float* __restrict__ mod,
                                               u16* __restrict__ x1, u16* __restrict__ h2) {
  extern __shared__ __attribute__((aligned(16))) char lds[];
  const int bm = blockIdx.x;
  EpiProj epi{x, mod, x1, h2, bm * 32};
  gemm_body<32, 576, 1, 2, 1, 6, 32, 4, 0>(lds, o, D_, bm * 32, 0, D_ / 32, nullptr, nullptr, nullptr, wo, wo, D_, epi);
}

struct EpiGLU {
  u16* hh; int m0, n0;
  template <int MT, int NTW, int NTHR>
  DI void run(f32x4 (&acc)[MT][NTW], char* lds, int row0, int tile0, int ntw, int fq) const {
    constexpr int RS = 592;
#pragma unroll
    for (int m = 0; m < MT; ++m)
#pragma unroll
      for (int n = 0; n < NTW; ++n) {
        u32x2 o;
        o[0] = pack2(acc[m][n][0], acc[m][n][1]);
        o[1] = pack2(acc[m][n][2], acc[m][n][3]);
        *reinterpret_cast<u32x2*>(lds + (row0 + m * 16) * RS + ((tile0 + n) * 16 + fq * 4) * 2) = o;
      }
    __syncthreads();
    for (int it = threadIdx.x; it < 256 * 18; it += NTHR) {
      const int row = it / 18, c = it % 18;
      const u32x4 g = *reinterpret_cast<const u32x4*>(lds + row * RS + c * 16);
      const u32x4 u = *reinterpret_cast<const u32x4*>(lds + row * RS + 288 + c * 16);
      u32x4 w;
#pragma unroll
      for (int j = 0; j < 4; ++j) w[j] = pack2(bflo(g[j]) * bflo(u[j]), bfhi(g[j]) * bfhi(u[j]));
      *reinterpret_cast<u32x4*>(hh + (size_t)(m0 + row) * F_ + n0 + c * 8) = w;
    }
  }
};

constexpr int LDS_GLU = 256 * 592;
__global__ __launch_bounds__(512) void k_glu(const u16* __restrict__ h2, const u16* __restrict__ wg,
                                              const u16* __restrict__ wu, u16* __restrict__ hh) {
  extern __shared__ __attribute__((aligned(16))) char lds[];
  const int b = blockIdx.x, xcd = b & 7, idx = b >> 3;
  const int bm = idx & 7, bn = xcd * 4 + (idx >> 3);
  const int n0 = bn * 144;
  EpiGLU epi{hh, bm * 256, n0};
  gemm_body<256, 288, 2, 4, 4, 2, 32, 4, 0>(lds, h2, D_, bm * 256, 0, D_ / 32, nullptr, nullptr, nullptr, wg + n0, wu + n0, F_, epi);
}

struct EpiPart {
  float* part; int m0, n0;
  template <int MT, int NTW, int NTHR>
  DI void run(f32x4 (&acc)[MT][NTW], char* lds, int row0, int tile0, int ntw, int fq) const {
    constexpr int RS = 592;
#pragma unroll
    for (int m = 0; m < MT; ++m)
#pragma unroll
      for (int n = 0; n < NTW; ++n)
        if (n < ntw) *reinterpret_cast<f32x4*>(lds + (row0 + m * 16) * RS + ((tile0 + n) * 16 + fq * 4) * 4) = acc[m][n];
    __syncthreads();
    for (int it = threadIdx.x; it < 128 * 36; it += NTHR) {
      const int row = it / 36, c = it % 36;
      f32x4 w = *reinterpret_cast<const f32x4*>(lds + row * RS + c * 16);
      *reinterpret_cast<f32x4*>(part + (size_t)(m0 + row) * D_ + n0 + c * 4) = w;
    }
  }
};

constexpr int LDS_DOWN = 4 * (128 * 64 * 2 + 64 * 144 * 2);
__global__ __launch_bounds__(512) void k_down(const u16* __restrict__ hh, const u16* __restrict__ wd,
                                               float* __restrict__ part) {
  extern __shared__ __attribute__((aligned(16))) char lds[];
  const int b = blockIdx.x, xcd = b & 7, idx = b >> 3;
  const int sk = xcd >> 1, bn = idx & 3, bm = (xcd & 1) * 8 + (idx >> 2);
  const int n0 = bn * 144;
  EpiPart epi{part + (size_t)sk * S_ * D_, bm * 128, n0};
  gemm_body<128, 144, 1, 2, 4, 2, 64, 4, 0>(lds, hh, F_, bm * 128, sk * (F_ / 4), F_ / 4 / 64, nullptr, nullptr, nullptr, wd + n0, wd + n0, D_, epi);
}

__global__ __launch_bounds__(256) void k_final(const float* __restrict__ part, const u16* __restrict__ x1,
                                                const float* __restrict__ mod, u16* __restrict__ out) {
  const int i = (blockIdx.x * 256 + threadIdx.x) * 8;
  if (i >= S_ * D_) return;
  const int col = i % D_;
  const float* gb = mod + 5 * D_ + col;
  u32x4 xv = *reinterpret_cast<const u32x4*>(x1 + i);
  u32x4 o;
#pragma unroll
  for (int j = 0; j < 4; ++j) {
    float a = 0.f, b = 0.f;
#pragma unroll
    for (int s = 0; s < 4; ++s) {
      f32x2 p = *reinterpret_cast<const f32x2*>(part + (size_t)s * S_ * D_ + i + 2 * j);
      a += p[0]; b += p[1];
    }
    o[j] = pack2(bflo(xv[j]) + gb[2 * j] * a, bfhi(xv[j]) + gb[2 * j + 1] * b);
  }
  *reinterpret_cast<u32x4*>(out + i) = o;
}

extern "C" void kernel_launch(void* const* d_in, const int* in_sizes, int n_in,
                              void* d_out, int out_size, void* d_ws, size_t ws_size,
                              hipStream_t stream) {
  const u16* x = (const u16*)d_in[0];
  const u16* wqkv = (const u16*)d_in[1];
  const u16* wo = (const u16*)d_in[2];
  const u16* wg = (const u16*)d_in[4];
  const u16* wu = (const u16*)d_in[5];
  const u16* wd = (const u16*)d_in[6];
  const u16* c = (const u16*)d_in[7];
  const u16* wmod = (const u16*)d_in[8];
  char* ws = (char*)d_ws;
  float* mod = (float*)(ws + WS_MOD);
  float2* st1 = (float2*)(ws + WS_ST1);
  u16* Q = (u16*)(ws + WS_Q);
  u16* K = (u16*)(ws + WS_K);
  u16* V = (u16*)(ws + WS_V);
  u16* O = (u16*)(ws + WS_O);
  u16* x1 = (u16*)(ws + WS_X1);
  u16* h2 = (u16*)(ws + WS_H2);
  u16* hh = (u16*)(ws + WS_HH);
  float* part = (float*)(ws + WS_PART);

  (void)hipFuncSetAttribute((const void*)k_qkv, hipFuncAttributeMaxDynamicSharedMemorySize, LDS_QKV);
  (void)hipFuncSetAttribute((const void*)k_proj, hipFuncAttributeMaxDynamicSharedMemorySize, LDS_PROJ);
  (void)hipFuncSetAttribute((const void*)k_glu, hipFuncAttributeMaxDynamicSharedMemorySize, LDS_GLU);
  (void)hipFuncSetAttribute((const void*)k_down, hipFuncAttributeMaxDynamicSharedMemorySize, LDS_DOWN);

  k_prep<<<108 + 512, 256, 0, stream>>>(c, wmod, x, mod, st1);
  k_qkv<<<192, 512, LDS_QKV, stream>>>(x, wqkv, st1, mod, Q);
  k_attn<<<32 * 16, 256, 0, stream>>>(Q, K, V, O);
  k_proj<<<64, 384, LDS_PROJ, stream>>>(O, wo, x, mod, x1, h2);
  k_glu<<<256, 512, LDS_GLU, stream>>>(h2, wg, wu, hh);
  k_down<<<256, 512, LDS_DOWN, stream>>>(hh, wd, part);
  k_final<<<(S_ * D_ / 8 + 255) / 256, 256, 0, stream>>>(part, x1, mod, (u16*)d_out);
}
```

```cpp
#include <hip/hip_runtime.h>
#include <hip/hip_bf16.h>
#include <stdint.h>
#include <type_traits>

typedef __attribute__((ext_vector_type(8))) short bf16x8;
typedef __attribute__((ext_vector_type(4))) short s16x4;
typedef __attribute__((ext_vector_type(4))) float f32x4;
typedef __attribute__((ext_vector_type(2))) float f32x2;
typedef __attribute__((ext_vector_type(16))) float f32x16;
typedef __attribute__((ext_vector_type(4))) unsigned u32x4;
typedef __attribute__((ext_vector_type(2))) unsigned u32x2;
typedef __attribute__((ext_vector_type(2))) __bf16 bf16x2_t;
typedef unsigned short u16;

#define DI __device__ __forceinline__

constexpr int S_ = 2048, D_ = 576, H_ = 32, DH_ = 18, F_ = 4608;
constexpr int HP = 32;
constexpr float QSCALE = 0.23570226039551584f * 1.4426950408889634f;

DI float bf2f(u16 v) { return __uint_as_float(((unsigned)v) << 16); }
DI float bflo(unsigned v) { return __uint_as_float(v << 16); }
DI float bfhi(unsigned v) { return __uint_as_float(v & 0xffff0000u); }
DI unsigned pack2(float a, float b) {
  f32x2 v = {a, b};
  bf16x2_t r = __builtin_convertvector(v, bf16x2_t);
  return __builtin_bit_cast(unsigned, r);
}
DI u16 f2bf(float a) { return (u16)(pack2(a, 0.f) & 0xffffu); }
DI float rbf(float a) { return bflo(pack2(a, 0.f) & 0xffffu); }

typedef __attribute__((address_space(3))) s16x4* lds_s16x4_ptr;
DI s16x4 tr_read(const char* p) {
  return __builtin_amdgcn_ds_read_tr16_b64_v4i16((lds_s16x4_ptr)(p));
}

constexpr size_t WS_MOD = 0;
constexpr size_t WS_ST1 = 16384;
constexpr size_t WS_ST2 = WS_ST1 + 2048 * 8;
constexpr size_t WS_Q = 1 << 20;
constexpr size_t WS_K = WS_Q + (size_t)H_ * S_ * HP * 2;
constexpr size_t WS_V = WS_K + (size_t)H_ * S_ * HP * 2;
constexpr size_t WS_O = WS_V + (size_t)H_ * S_ * HP * 2;
constexpr size_t WS_X1 = WS_O + (size_t)S_ * D_ * 2;
constexpr size_t WS_H2 = WS_X1 + (size_t)S_ * D_ * 2;
constexpr size_t WS_HH = WS_H2 + (size_t)S_ * D_ * 2;
constexpr size_t WS_PART = WS_HH + (size_t)S_ * F_ * 2;

__global__ __launch_bounds__(256) void k_prep(const u16* __restrict__ c, const u16* __restrict__ wmod,
                                               const u16* __restrict__ x, float* __restrict__ mod,
                                               float2* __restrict__ st1) {
  __shared__ float red[64][33];
  const int t = threadIdx.x;
  if (blockIdx.x < 108) {
    const int n0 = blockIdx.x * 32 + (t & 3) * 8;
    const int ks = t >> 2;
    float acc[8];
#pragma unroll
    for (int j = 0; j < 8; ++j) acc[j] = 0.f;
#pragma unroll
    for (int r = 0; r < 9; ++r) {
      const int k = ks * 9 + r;
      float cv = bf2f(c[k]);
      float sv = rbf(cv / (1.f + __expf(-cv)));
      u32x4 w = *reinterpret_cast<const u32x4*>(wmod + (size_t)k * 3456 + n0);
#pragma unroll
      for (int j = 0; j < 4; ++j) {
        acc[2 * j] += sv * bflo(w[j]);
        acc[2 * j + 1] += sv * bfhi(w[j]);
      }
    }
#pragma unroll
    for (int j = 0; j < 8; ++j) red[ks][(t & 3) * 8 + j] = acc[j];
    __syncthreads();
    if (t < 32) {
      float s = 0.f;
#pragma unroll 8
      for (int i = 0; i < 64; ++i) s += red[i][t];
      mod[blockIdx.x * 32 + t] = rbf(s);
    }
  } else {
    const int lane = t & 63, w = t >> 6;
    for (int row = (blockIdx.x - 108) * 4 + w; row < S_; row += (gridDim.x - 108) * 4) {
      const u16* xr = x + (size_t)row * D_;
      u32x4 a = *reinterpret_cast<const u32x4*>(xr + lane * 8);
      u32x4 b = {0, 0, 0, 0};
      if (lane < 8) b = *reinterpret_cast<const u32x4*>(xr + 512 + lane * 8);
      float v[16];
#pragma unroll
      for (int j = 0; j < 4; ++j) {
        v[2 * j] = bflo(a[j]); v[2 * j + 1] = bfhi(a[j]);
        v[8 + 2 * j] = bflo(b[j]); v[8 + 2 * j + 1] = bfhi(b[j]);
      }
      float s = 0.f;
#pragma unroll
      for (int j = 0; j < 16; ++j) s += v[j];
#pragma unroll
      for (int o = 32; o >= 1; o >>= 1) s += __shfl_xor(s, o);
      const float mean = s * (1.f / D_);
      float q = 0.f;
#pragma unroll
      for (int j = 0; j < 8; ++j) { float d = v[j] - mean; q += d * d; }
      if (lane < 8) {
#pragma unroll
        for (int j = 8; j < 16; ++j) { float d = v[j] - mean; q += d * d; }
      }
#pragma unroll
      for (int o = 32; o >= 1; o >>= 1) q += __shfl_xor(q, o);
      if (lane == 0) st1[row] = make_float2(mean, rsqrtf(q * (1.f / D_) + 1e-5f));
    }
  }
}

#define WAIT_LGKM0() asm volatile("s_waitcnt lgkmcnt(0)" ::: "memory")
#define STAMP(i)
#define RAW_BAR() __builtin_amdgcn_s_barrier()
template <int N> DI void wait_vm() { asm volatile("s_waitcnt vmcnt(%0)" ::"n"(N) : "memory"); }
template <int OFF> DI void lds_tr(u32x2& d, unsigned a) { asm volatile("ds_read_b64_tr_b16 %0, %1 offset:%2" : "=v"(d) : "v"(a), "n"(OFF)); }
template <int OFF> DI void lds_128(u32x4& d, unsigned a) { asm volatile("ds_read_b128 %0, %1 offset:%2" : "=v"(d) : "v"(a), "n"(OFF)); }
DI void pin4(u32x4& a) { asm volatile("" : "+v"(a)); }
template <int N> DI void wait_l2(u32x2& a, u32x2& b) { asm volatile("s_waitcnt lgkmcnt(%2)" : "+v"(a), "+v"(b) : "n"(N)); }
template <int N> DI void wait_l4(u32x2& a, u32x2& b, u32x4& c, u32x4& d) { asm volatile("s_waitcnt lgkmcnt(%4)" : "+v"(a), "+v"(b), "+v"(c), "+v"(d) : "n"(N)); }

template <int BK> DI int a_swz(int row) { return BK >= 64 ? ((row >> 1) & 7) : ((-(row >> 2)) & 3); }

template <int I, int N, class F> DI void static_for(F&& f) {
  if constexpr (I < N) { f(std::integral_constant<int, I>{}); static_for<I + 1, N>(f); }
}

template <int BM, int BN, int NIMG, int MT, int WM, int WN, int BK, int NST, int LNMODE  , class Epi>
DI void gemm_body(char* lds, const u16* __restrict__ A, int lda, int m0, int kbeg, int nk,
                  const float2* __restrict__ stats, const float* __restrict__ mscale, const float* __restrict__ mshift,
                  const u16* __restrict__ B0, const u16* __restrict__ B1, int ldb, Epi& epi) {
  constexpr int NW = WM * WN, NTHR = 64 * NW;
  constexpr int NT = BN / 16, NTW0 = (NT + WN - 1) / WN, NTWL = NT - (WN - 1) * NTW0, BNI = BN / NIMG, NTI = NT / NIMG;
  constexpr int ABYTES = BM * BK * 2, BBYTES = BK * BN * 2, STAGE = ABYTES + BBYTES;
  constexpr int AINS = ABYTES / 1024, BINS = BBYTES / 1024;
  constexpr int BINS_I = BINS / NIMG;
  constexpr int TINS = AINS + BINS;
  constexpr int G = (TINS + NW - 1) / NW;
  constexpr int SUB = BK / 32;
  constexpr int CPR = BK >= 64 ? 8 : 4;
  constexpr int ARB = CPR * 16;
  static_assert(BBYTES % (1024 * NIMG) == 0 && ABYTES % 1024 == 0 && BM == WM * MT * 16, "tile");
  static_assert(NIMG == 1 || NTW0 == NTI, "img");
  const int t = threadIdx.x, lane = t & 63;
  const int wid = __builtin_amdgcn_readfirstlane(t >> 6);
  const int wr = wid % WM, wn = wid / WM, fr = lane & 15, fq = lane >> 4;
  float* lnp = reinterpret_cast<float*>(lds + NST * STAGE);

  STAMP(0);
  unsigned goff[G];
#pragma unroll
  for (int ii = 0; ii < G; ++ii) {
    int i = wid + ii * NW;
    if (i >= TINS) i -= TINS;
    if (i < AINS) {
      const int j = i * 64 + lane;
      const int si = j / (BM * CPR), row = (j / CPR) % BM, pos = j % CPR;
      goff[ii] = (unsigned)((row * lda + si * 64 + (pos ^ a_swz<BK>(row)) * 8) * 2);
    } else {
      const int j = ((i - AINS) % BINS_I) * 64 + lane;
      const int kb = j / BNI, rem = j % BNI;
      const int sub = rem >> 4, r = (rem >> 1) & 7, hf = rem & 1;
      goff[ii] = (unsigned)(((kb * 8 + (r ^ ((kb & 1) << 2))) * ldb + sub * 16 + hf * 8) * 2);
    }
  }
  const char* Abase = (const char*)(A + (size_t)m0 * lda + kbeg);
  const char* B0base = (const char*)(B0 + (size_t)kbeg * ldb);
  const char* B1base = (const char*)(B1 + (size_t)kbeg * ldb);

  auto issue_one = [&](int kt, int ii) {
    char* buf = lds + (kt % NST) * STAGE;
    int i = wid + ii * NW;
    if (i >= TINS) i -= TINS;
    const char* base = (i < AINS) ? (Abase + kt * BK * 2)
                                  : (((NIMG == 2 && i - AINS >= BINS_I) ? B1base : B0base) + (size_t)kt * BK * ldb * 2);
    __builtin_amdgcn_global_load_lds((const unsigned*)(base + goff[ii]),
                                     (__attribute__((address_space(3))) unsigned*)(buf + (i * 64 + lane) * 16), 16, 0, 0);
  };

#pragma unroll
  for (int st = 0; st < NST - 1; ++st)
    if (st < nk) {
#pragma unroll
      for (int ii = 0; ii < G; ++ii) issue_one(st, ii);
    }

  float ra[MT], rb[MT];
  if constexpr (LNMODE != 0) {
    for (int i = t; i < D_; i += NTHR) { lnp[i] = mscale[i] + 1.f; lnp[D_ + i] = mshift[i]; }
#pragma unroll
    for (int m = 0; m < MT; ++m) {
      float2 s = stats[m0 + (wr * MT + m) * 16 + fr];
      ra[m] = s.y; rb[m] = -s.x * s.y;
    }
    wait_vm<0>();
    WAIT_LGKM0();
  }

  f32x4 acc[MT][NTW0];
#pragma unroll
  for (int m = 0; m < MT; ++m)
#pragma unroll
    for (int n = 0; n < NTW0; ++n) acc[m][n] = f32x4{0.f, 0.f, 0.f, 0.f};

  const unsigned lbase = (unsigned)(size_t)lds;
  const int q4 = fr >> 2, p4 = lane & 3, sw = (fq & 1) << 2;
  const int arow = wr * MT * 16 + fr;
  const unsigned aoffb = arow * ARB;
  const int tile0 = wn * NTW0;
  const unsigned bimg = (unsigned)((tile0 / NTI) * (BBYTES / NIMG) + (tile0 % NTI) * 256);
  const unsigned boff_lo = ABYTES + bimg + fq * (BNI * 16) + ((q4 ^ sw) << 5) + p4 * 8;
  const unsigned boff_hi = ABYTES + bimg + fq * (BNI * 16) + (((q4 + 4) ^ sw) << 5) + p4 * 8;

  auto mainloop = [&](auto NTWC) {
    constexpr int NTW = decltype(NTWC)::value;
    constexpr int PF = 3;
    constexpr int T = SUB * NTW;
    for (int kt = 0; kt < nk; ++kt) {
      STAMP(2 + 2 * kt);
      if (kt + NST - 2 < nk) wait_vm<G * (NST - 2)>(); else wait_vm<0>();
      RAW_BAR();
      STAMP(3 + 2 * kt);
      const bool more = kt + NST - 1 < nk;
      const unsigned sb = lbase + (kt % NST) * STAGE;
      const unsigned vb_lo = sb + boff_lo, vb_hi = sb + boff_hi;
      u32x2 blo[PF], bhi[PF];
      u32x4 af[2][MT];
      auto rda = [&](auto S2) {
        constexpr int s2 = decltype(S2)::value;
        const int ch = (s2 & (CPR / 4 - 1)) * 4 + fq;
        const unsigned base = sb + (s2 / (CPR / 4)) * (BM * ARB) + aoffb;
        lds_128<0>(af[s2 & 1][0], base + ((ch ^ a_swz<BK>(arow)) << 4));
        if constexpr (MT > 1) lds_128<16 * ARB>(af[s2 & 1][MT > 1 ? 1 : 0], base + ((ch ^ a_swz<BK>(arow + 16)) << 4));
        if constexpr (MT > 2) {
          lds_128<32 * ARB>(af[s2 & 1][MT > 2 ? 2 : 0], base + ((ch ^ a_swz<BK>(arow + 32)) << 4));
          lds_128<48 * ARB>(af[s2 & 1][MT > 2 ? 3 : 0], base + ((ch ^ a_swz<BK>(arow + 48)) << 4));
        }
      };
      auto rdb = [&](auto IDX) {
        constexpr int idx = decltype(IDX)::value;
        constexpr int s2 = idx / NTW, n = idx % NTW;
        constexpr int off = s2 * 4 * (BNI * 16) + n * 256;
        lds_tr<off>(blo[idx % PF], vb_lo);
        lds_tr<off>(bhi[idx % PF], vb_hi);
      };
      rda(std::integral_constant<int, 0>{});
      static_for<0, PF - 1>([&](auto I) { rdb(I); });
      static_for<0, T>([&](auto IDX) {
        constexpr int idx = decltype(IDX)::value;
        constexpr int s2 = idx / NTW, n = idx % NTW;
        if constexpr (n == NTW - PF && s2 + 1 < SUB) rda(std::integral_constant<int, s2 + 1>{});
        if constexpr (idx + PF - 1 < T) rdb(std::integral_constant<int, idx + PF - 1>{});
        constexpr int nb = ((idx + PF - 1 < T) ? (idx + PF - 1) : (T - 1)) - idx;
        constexpr int sA = (idx + PF) / NTW - 1;
        constexpr int idxA = sA * NTW + NTW - PF;
        constexpr bool a_after = (sA >= 0) && (sA + 1 < SUB) && (idxA <= idx) && (idx <= idxA + PF - 2);
        constexpr int cnt = 2 * nb + (a_after ? MT : 0);
        if constexpr (n == 0) {
          asm volatile("s_waitcnt lgkmcnt(%0)" ::"n"(cnt));
          pin4(af[s2 & 1][0]);
          if constexpr (MT > 1) pin4(af[s2 & 1][MT > 1 ? 1 : 0]);
          if constexpr (MT > 2) { pin4(af[s2 & 1][MT - 2]); pin4(af[s2 & 1][MT - 1]); }
          asm volatile("" : "+v"(blo[idx % PF]), "+v"(bhi[idx % PF]));
          if constexpr (LNMODE != 0) {
            const int kc = kbeg + kt * BK + s2 * 32 + fq * 8;
            f32x4 sc0 = *reinterpret_cast<const f32x4*>(lnp + kc), sc1 = *reinterpret_cast<const f32x4*>(lnp + kc + 4);
            f32x4 sh0 = *reinterpret_cast<const f32x4*>(lnp + D_ + kc), sh1 = *reinterpret_cast<const f32x4*>(lnp + D_ + kc + 4);
#pragma unroll
            for (int m = 0; m < MT; ++m) {
              u32x4 r = af[s2 & 1][m], o;
              const float a = ra[m], b = rb[m];
              o[0] = pack2((bflo(r[0]) * a + b) * sc0[0] + sh0[0], (bfhi(r[0]) * a + b) * sc0[1] + sh0[1]);
              o[1] = pack2((bflo(r[1]) * a + b) * sc0[2] + sh0[2], (bfhi(r[1]) * a + b) * sc0[3] + sh0[3]);
              o[2] = pack2((bflo(r[2]) * a + b) * sc1[0] + sh1[0], (bfhi(r[2]) * a + b) * sc1[1] + sh1[1]);
              o[3] = pack2((bflo(r[3]) * a + b) * sc1[2] + sh1[2], (bfhi(r[3]) * a + b) * sc1[3] + sh1[3]);
              af[s2 & 1][m] = o;
            }
          }
        } else {
          wait_l2<cnt>(blo[idx % PF], bhi[idx % PF]);
        }
        u32x4 bw = {blo[idx % PF][0], blo[idx % PF][1], bhi[idx % PF][0], bhi[idx % PF][1]};
        const bf16x8 bf = __builtin_bit_cast(bf16x8, bw);
#pragma unroll
        for (int m = 0; m < MT; ++m)
          acc[m][n] = __builtin_amdgcn_mfma_f32_16x16x32_bf16(bf, __builtin_bit_cast(bf16x8, af[s2 & 1][m]), acc[m][n], 0, 0, 0);
        static_for<0, G>([&](auto II) {
          constexpr int ii = decltype(II)::value;
          if constexpr (idx == (ii * T) / G) { if (more) issue_one(kt + NST - 1, ii); }
        });
      });
    }
  };
  if constexpr (NTWL != NTW0) {
    if (wn == WN - 1) mainloop(std::integral_constant<int, NTWL>{});
    else mainloop(std::integral_constant<int, NTW0>{});
  } else {
    mainloop(std::integral_constant<int, NTW0>{});
  }
  STAMP(50);
  __syncthreads();
  STAMP(51);
  const int ntw = (wn == WN - 1) ? NTWL : NTW0;
  epi.template run<MT, NTW0, NTHR>(acc, lds, wr * MT * 16 + fr, tile0, ntw, fq);
  STAMP(52);
}

struct EpiQKV {
  u16* qkv;
  int m0, n0;
  template <int MT, int NTW, int NTHR>
  DI void run(f32x4 (&acc)[MT][NTW], char* lds, int row0, int tile0, int ntw, int fq) const {
    constexpr int RS = 296;
    const int which = n0 / D_;
    const float sc = which == 0 ? QSCALE : 1.f;
#pragma unroll
    for (int m = 0; m < MT; ++m)
#pragma unroll
      for (int n = 0; n < NTW; ++n) {
        if (n < ntw) {
          u32x2 o;
          o[0] = pack2(acc[m][n][0] * sc, acc[m][n][1] * sc);
          o[1] = pack2(acc[m][n][2] * sc, acc[m][n][3] * sc);
          *reinterpret_cast<u32x2*>(lds + (row0 + m * 16) * RS + ((tile0 + n) * 16 + fq * 4) * 2) = o;
        }
      }
    __syncthreads();
    const int head0 = (n0 % D_) / DH_;
    const unsigned padw = (which == 2) ? 0x00003f80u : 0u;
    for (int it = threadIdx.x; it < 8 * 128 * 4; it += NTHR) {
      const int ch = it & 3, row = (it >> 2) & 127, hd = it >> 9;
      const unsigned* src = reinterpret_cast<const unsigned*>(lds + row * RS + hd * 36 + ch * 16);
      u32x4 w = {0u, 0u, 0u, 0u};
      if (ch < 2) w = u32x4{src[0], src[1], src[2], src[3]};
      else if (ch == 2) w = u32x4{src[0], padw, 0u, 0u};
      *reinterpret_cast<u32x4*>(qkv + ((size_t)(which * H_ + head0 + hd) * S_ + m0 + row) * HP + ch * 8) = w;
    }
  }
};

constexpr int LDS_QKV = 4 * (128 * 64 * 2 + 64 * 144 * 2) + 2 * D_ * 4;
__global__ __launch_bounds__(512) void k_qkv(const u16* __restrict__ x, const u16* __restrict__ wqkv,
                                              const float2* __restrict__ st1, const float* __restrict__ mod,
                                              u16* __restrict__ qkv) {
  extern __shared__ __attribute__((aligned(16))) char lds[];
  const int b = blockIdx.x;
  const int xcd = b & 7, idx = b >> 3;
  const int bn = (idx % 3) * 4 + (xcd >> 1), bm = (idx / 3) * 2 + (xcd & 1);
  const int n0 = bn * 144;
  EpiQKV epi{qkv, bm * 128, n0};
  gemm_body<128, 144, 1, 2, 4, 2, 64, 4, 1>(lds, x, D_, bm * 128, 0, D_ / 64, st1, mod + D_, mod, wqkv + n0, wqkv + n0, 3 * D_, epi);
}

__global__ __launch_bounds__(512, 4) void k_attn(const u16* __restrict__ Q, const u16* __restrict__ K,
                                                  const u16* __restrict__ V, u16* __restrict__ O) {
  __shared__ __attribute__((aligned(16))) char lds_all[2 * 3 * 8192];
  const int t = threadIdx.x, lane = t & 63, wid = t >> 6, r32 = lane & 31, h = lane >> 5;
  const int half = wid >> 2, tl = t & 255;
  char* lds = lds_all + half * (3 * 8192);
  const int head = blockIdx.x >> 4, qb = blockIdx.x & 15;
  const int q0 = qb * 128 + (wid & 3) * 32;
  const u16* Qh = Q + (size_t)head * S_ * HP;
  const u16* Kh = K + ((size_t)head * S_ + half * 1024) * HP;
  const u16* Vh = V + ((size_t)head * S_ + half * 1024) * HP;
  const bf16x8 qf0 = *reinterpret_cast<const bf16x8*>(Qh + (size_t)(q0 + r32) * HP + 8 * h);
  const bf16x8 qf1 = *reinterpret_cast<const bf16x8*>(Qh + (size_t)(q0 + r32) * HP + 16 + 8 * h);

  const int skey = tl >> 2, sch = tl & 3;
  const int kst_off = skey * 64 + ((sch ^ ((skey >> 2) & 3)) << 4);
  const int vst_off = 4096 + skey * 64 + sch * 16;
  u32x4 kreg, vreg;
  auto gload = [&](int st) {
    kreg = *reinterpret_cast<const u32x4*>(Kh + (size_t)(st * 64 + skey) * HP + sch * 8);
    vreg = *reinterpret_cast<const u32x4*>(Vh + (size_t)(st * 64 + skey) * HP + sch * 8);
  };
  auto swrite = [&](char* buf) {
    *reinterpret_cast<u32x4*>(buf + kst_off) = kreg;
    *reinterpret_cast<u32x4*>(buf + vst_off) = vreg;
  };

  f32x16 oacc, negm;
#pragma unroll
  for (int i = 0; i < 16; ++i) { oacc[i] = 0.f; negm[i] = 0.f; }
  constexpr float THR = 40.f;

  const int kr0 = r32 * 64 + (((0 + h) ^ ((r32 >> 2) & 3)) << 4);
  const int kr1 = r32 * 64 + (((2 + h) ^ ((r32 >> 2) & 3)) << 4);
  const int g = lane >> 4, q4 = (lane & 15) >> 2, p4 = lane & 3;
  const int vr = 4096 + (4 * (g >> 1) + q4) * 64 + (g & 1) * 32 + p4 * 8;

  constexpr int NS = 1024 / 64;
  gload(0); swrite(lds);
  gload(1); swrite(lds + 8192);
  __syncthreads();
  auto qk = [&](const char* buf, int u) -> f32x16 {
    const bf16x8 ka = *reinterpret_cast<const bf16x8*>(buf + u * 2048 + kr0);
    const bf16x8 kb = *reinterpret_cast<const bf16x8*>(buf + u * 2048 + kr1);
    f32x16 s = __builtin_amdgcn_mfma_f32_32x32x16_bf16(ka, qf0, negm, 0, 0, 0);
    return __builtin_amdgcn_mfma_f32_32x32x16_bf16(kb, qf1, s, 0, 0, 0);
  };
  f32x16 sc = qk(lds, 0);
  int bcur = 0;
  for (int st = 0; st < NS; ++st) {
    const char* cur = lds + bcur * 8192;
    const int bn1 = bcur == 2 ? 0 : bcur + 1, bn2 = bn1 == 2 ? 0 : bn1 + 1;
    const char* nx1 = lds + bn1 * 8192;
    if (st + 2 < NS) gload(st + 2);
#pragma unroll
    for (int u = 0; u < 2; ++u) {
      f32x16 sn;
      if (u < 1) sn = qk(cur, u + 1);
      else sn = qk(nx1, 0);
      float mx = fmaxf(sc[0], sc[1]);
#pragma unroll
      for (int i = 2; i < 16; ++i) mx = fmaxf(mx, sc[i]);
      const bool first = (st == 0 && u == 0);
      if (first || !__all(mx <= THR)) {
        mx = fmaxf(mx, __shfl_xor(mx, 32));
        const float d = first ? mx : fmaxf(mx, 0.f);
        const float alpha = first ? 0.f : __builtin_amdgcn_exp2f(-d);
#pragma unroll
        for (int i = 0; i < 16; ++i) { oacc[i] *= alpha; negm[i] -= d; sc[i] -= d; sn[i] -= d; }
      }
      u32x4 pf0, pf1;
#pragma unroll
      for (int i = 0; i < 4; ++i) {
        pf0[i] = pack2(__builtin_amdgcn_exp2f(sc[2 * i]), __builtin_amdgcn_exp2f(sc[2 * i + 1]));
        pf1[i] = pack2(__builtin_amdgcn_exp2f(sc[8 + 2 * i]), __builtin_amdgcn_exp2f(sc[8 + 2 * i + 1]));
      }
      {
        s16x4 lo = tr_read(cur + vr + (32 * u) * 64);
        s16x4 hi = tr_read(cur + vr + (32 * u + 8) * 64);
        s16x4 lo2 = tr_read(cur + vr + (32 * u + 16) * 64);
        s16x4 hi2 = tr_read(cur + vr + (32 * u + 24) * 64);
        const bf16x8 vf0 = __builtin_shufflevector(lo, hi, 0, 1, 2, 3, 4, 5, 6, 7);
        const bf16x8 vf1 = __builtin_shufflevector(lo2, hi2, 0, 1, 2, 3, 4, 5, 6, 7);
        oacc = __builtin_amdgcn_mfma_f32_32x32x16_bf16(vf0, __builtin_bit_cast(bf16x8, pf0), oacc, 0, 0, 0);
        oacc = __builtin_amdgcn_mfma_f32_32x32x16_bf16(vf1, __builtin_bit_cast(bf16x8, pf1), oacc, 0, 0, 0);
      }
      sc = sn;
    }
    if (st + 2 < NS) swrite(lds + bn2 * 8192);
    __syncthreads();
    bcur = bn1;
  }
  float* xch = reinterpret_cast<float*>(lds_all);
  if (half == 1) {
#pragma unroll
    for (int i = 0; i < 11; ++i) xch[(i * 4 + (wid & 3)) * 64 + lane] = oacc[i];
    xch[(11 * 4 + (wid & 3)) * 64 + lane] = negm[0];
  }
  __syncthreads();
  if (half == 1) return;
  {
    const float nm1 = xch[(11 * 4 + wid) * 64 + lane], nm0 = negm[0];
    const float nm = fminf(nm0, nm1);
    const float f0 = __builtin_amdgcn_exp2f(nm - nm0), f1 = __builtin_amdgcn_exp2f(nm - nm1);
#pragma unroll
    for (int i = 0; i < 11; ++i) oacc[i] = oacc[i] * f0 + xch[(i * 4 + wid) * 64 + lane] * f1;
  }
  const float l = __shfl(oacc[10], r32);
  const float inv = 1.f / l;
  unsigned* op = reinterpret_cast<unsigned*>(O + (size_t)(q0 + r32) * D_ + head * DH_);
  const unsigned w0 = pack2(oacc[0] * inv, oacc[1] * inv), w1 = pack2(oacc[2] * inv, oacc[3] * inv);
  const unsigned w2 = pack2(oacc[4] * inv, oacc[5] * inv), w3 = pack2(oacc[6] * inv, oacc[7] * inv);
  const unsigned w4 = pack2(oacc[8] * inv, oacc[9] * inv);
  op[2 * h] = w0; op[2 * h + 1] = w1; op[4 + 2 * h] = w2; op[5 + 2 * h] = w3;
  if (h == 0) op[8] = w4;
}

struct EpiProj {
  const u16* x; const float* ga; u16* x1; float2* st2; int m0, n0;
  template <int MT, int NTW, int NTHR>
  DI void run(f32x4 (&acc)[MT][NTW], char* lds, int row0, int tile0, int ntw, int fq) const {
    const int part = (n0 / 144) * 4 + tile0 / NTW;
#pragma unroll
    for (int m = 0; m < MT; ++m) {
      const int row = m0 + row0 + m * 16;
      float s = 0.f;
#pragma unroll
      for (int n = 0; n < NTW; ++n) {
        if (n < ntw) {
          const int col = n0 + (tile0 + n) * 16 + fq * 4;
          u32x2 xv = *reinterpret_cast<const u32x2*>(x + (size_t)row * D_ + col);
          f32x4 g = *reinterpret_cast<const f32x4*>(ga + col);
          u32x2 o;
          o[0] = pack2(bflo(xv[0]) + g[0] * acc[m][n][0], bfhi(xv[0]) + g[1] * acc[m][n][1]);
          o[1] = pack2(bflo(xv[1]) + g[2] * acc[m][n][2], bfhi(xv[1]) + g[3] * acc[m][n][3]);
          *reinterpret_cast<u32x2*>(x1 + (size_t)row * D_ + col) = o;
          acc[m][n] = f32x4{bflo(o[0]), bfhi(o[0]), bflo(o[1]), bfhi(o[1])};
          s += (acc[m][n][0] + acc[m][n][1]) + (acc[m][n][2] + acc[m][n][3]);
        }
      }
      s += __shfl_xor(s, 16); s += __shfl_xor(s, 32);
      const float lm = s / (float)(16 * ntw);
      float q = 0.f;
#pragma unroll
      for (int n = 0; n < NTW; ++n)
        if (n < ntw) {
#pragma unroll
          for (int j = 0; j < 4; ++j) { float d = acc[m][n][j] - lm; q += d * d; }
        }
      q += __shfl_xor(q, 16); q += __shfl_xor(q, 32);
      if (fq == 0) st2[(size_t)row * 16 + part] = make_float2(s, q);
    }
  }
};

constexpr int LDS_PROJ = 4 * (32 * 64 * 2 + 64 * 144 * 2);
__global__ __launch_bounds__(192) void k_proj(const u16* __restrict__ o, const u16* __restrict__ wo,
                                               const u16* __restrict__ x, const float* __restrict__ mod,
                                               u16* __restrict__ x1, float2* __restrict__ st2) {
  extern __shared__ __attribute__((aligned(16))) char lds[];
  const int b = blockIdx.x;
  const int bn = b & 3, bm = b >> 2;
  const int n0 = bn * 144;
  EpiProj epi{x, mod + 2 * D_, x1, st2, bm * 32, n0};
  gemm_body<32, 144, 1, 2, 1, 3, 64, 4, 0>(lds, o, D_, bm * 32, 0, D_ / 64, nullptr, nullptr, nullptr, wo + n0, wo + n0, D_, epi);
}

__global__ __launch_bounds__(256) void k_ln2(const u16* __restrict__ x1, const float2* __restrict__ st2,
                                              const float* __restrict__ mod, u16* __restrict__ h2) {
  const int row = blockIdx.x * 4 + (threadIdx.x >> 6), lane = threadIdx.x & 63;
  const float2 pp = st2[(size_t)row * 16 + (lane & 15)];
  const float cnt = ((lane & 3) == 3) ? 0.f : 48.f;
  float s = (cnt > 0.f) ? pp.x : 0.f;
#pragma unroll
  for (int o = 8; o >= 1; o >>= 1) s += __shfl_xor(s, o);
  const float mean = s * (1.f / D_);
  float d = (cnt > 0.f) ? (pp.x / 48.f - mean) : 0.f;
  float m2 = (cnt > 0.f) ? (pp.y + cnt * d * d) : 0.f;
#pragma unroll
  for (int o = 8; o >= 1; o >>= 1) m2 += __shfl_xor(m2, o);
  const float a = rsqrtf(m2 * (1.f / D_) + 1e-5f), b = -mean * a;
  const float* sb = mod + 3 * D_;
  const float* cb = mod + 4 * D_;
  for (int c = lane; c < 72; c += 64) {
    u32x4 r = *reinterpret_cast<const u32x4*>(x1 + (size_t)row * D_ + c * 8);
    f32x4 sc0 = *reinterpret_cast<const f32x4*>(cb + c * 8), sc1 = *reinterpret_cast<const f32x4*>(cb + c * 8 + 4);
    f32x4 sh0 = *reinterpret_cast<const f32x4*>(sb + c * 8), sh1 = *reinterpret_cast<const f32x4*>(sb + c * 8 + 4);
    u32x4 o;
    o[0] = pack2((bflo(r[0]) * a + b) * (sc0[0] + 1.f) + sh0[0], (bfhi(r[0]) * a + b) * (sc0[1] + 1.f) + sh0[1]);
    o[1] = pack2((bflo(r[1]) * a + b) * (sc0[2] + 1.f) + sh0[2], (bfhi(r[1]) * a + b) * (sc0[3] + 1.f) + sh0[3]);
    o[2] = pack2((bflo(r[2]) * a + b) * (sc1[0] + 1.f) + sh1[0], (bfhi(r[2]) * a + b) * (sc1[1] + 1.f) + sh1[1]);
    o[3] = pack2((bflo(r[3]) * a + b) * (sc1[2] + 1.f) + sh1[2], (bfhi(r[3]) * a + b) * (sc1[3] + 1.f) + sh1[3]);
    *reinterpret_cast<u32x4*>(h2 + (size_t)row * D_ + c * 8) = o;
  }
}

struct EpiGLU {
  u16* hh; int m0, n0;
  template <int MT, int NTW, int NTHR>
  DI void run(f32x4 (&acc)[MT][NTW], char* lds, int row0, int tile0, int ntw, int fq) const {
    constexpr int RS = 592;
#pragma unroll
    for (int m = 0; m < MT; ++m)
#pragma unroll
      for (int n = 0; n < NTW; ++n) {
        u32x2 o;
        o[0] = pack2(acc[m][n][0], acc[m][n][1]);
        o[1] = pack2(acc[m][n][2], acc[m][n][3]);
        *reinterpret_cast<u32x2*>(lds + (row0 + m * 16) * RS + ((tile0 + n) * 16 + fq * 4) * 2) = o;
      }
    __syncthreads();
    for (int it = threadIdx.x; it < 256 * 18; it += NTHR) {
      const int row = it / 18, c = it % 18;
      const u32x4 g = *reinterpret_cast<const u32x4*>(lds + row * RS + c * 16);
      const u32x4 u = *reinterpret_cast<const u32x4*>(lds + row * RS + 288 + c * 16);
      u32x4 w;
#pragma unroll
      for (int j = 0; j < 4; ++j) w[j] = pack2(bflo(g[j]) * bflo(u[j]), bfhi(g[j]) * bfhi(u[j]));
      *reinterpret_cast<u32x4*>(hh + (size_t)(m0 + row) * F_ + n0 + c * 8) = w;
    }
  }
};

constexpr int LDS_GLU = 256 * 592;
__global__ __launch_bounds__(512) void k_glu(const u16* __restrict__ h2, const u16* __restrict__ wg,
                                              const u16* __restrict__ wu, u16* __restrict__ hh) {
  extern __shared__ __attribute__((aligned(16))) char lds[];
  const int b = blockIdx.x, xcd = b & 7, idx = b >> 3;
  const int bm = idx & 7, bn = xcd * 4 + (idx >> 3);
  const int n0 = bn * 144;
  EpiGLU epi{hh, bm * 256, n0};
  gemm_body<256, 288, 2, 4, 4, 2, 32, 4, 0>(lds, h2, D_, bm * 256, 0, D_ / 32, nullptr, nullptr, nullptr, wg + n0, wu + n0, F_, epi);
}

struct EpiPart {
  float* part; int m0, n0;
  template <int MT, int NTW, int NTHR>
  DI void run(f32x4 (&acc)[MT][NTW], char* lds, int row0, int tile0, int ntw, int fq) const {
    constexpr int RS = 592;
#pragma unroll
    for (int m = 0; m < MT; ++m)
#pragma unroll
      for (int n = 0; n < NTW; ++n)
        if (n < ntw) *reinterpret_cast<f32x4*>(lds + (row0 + m * 16) * RS + ((tile0 + n) * 16 + fq * 4) * 4) = acc[m][n];
    __syncthreads();
    for (int it = threadIdx.x; it < 128 * 36; it += NTHR) {
      const int row = it / 36, c = it % 36;
      f32x4 w = *reinterpret_cast<const f32x4*>(lds + row * RS + c * 16);
      *reinterpret_cast<f32x4*>(part + (size_t)(m0 + row) * D_ + n0 + c * 4) = w;
    }
  }
};

constexpr int LDS_DOWN = 4 * (128 * 64 * 2 + 64 * 144 * 2);
__global__ __launch_bounds__(512) void k_down(const u16* __restrict__ hh, const u16* __restrict__ wd,
                                               float* __restrict__ part) {
  extern __shared__ __attribute__((aligned(16))) char lds[];
  const int b = blockIdx.x, xcd = b & 7, idx = b >> 3;
  const int sk = xcd >> 1, bn = idx & 3, bm = (xcd & 1) * 8 + (idx >> 2);
  const int n0 = bn * 144;
  EpiPart epi{part + (size_t)sk * S_ * D_, bm * 128, n0};
  gemm_body<128, 144, 1, 2, 4, 2, 64, 4, 0>(lds, hh, F_, bm * 128, sk * (F_ / 4), F_ / 4 / 64, nullptr, nullptr, nullptr, wd + n0, wd + n0, D_, epi);
}

__global__ __launch_bounds__(256) void k_final(const float* __restrict__ part, const u16* __restrict__ x1,
                                                const float* __restrict__ mod, u16* __restrict__ out) {
  const int i = (blockIdx.x * 256 + threadIdx.x) * 8;
  if (i >= S_ * D_) return;
  const int col = i % D_;
  const float* gb = mod + 5 * D_ + col;
  u32x4 xv = *reinterpret_cast<const u32x4*>(x1 + i);
  u32x4 o;
#pragma unroll
  for (int j = 0; j < 4; ++j) {
    float a = 0.f, b = 0.f;
#pragma unroll
    for (int s = 0; s < 4; ++s) {
      f32x2 p = *reinterpret_cast<const f32x2*>(part + (size_t)s * S_ * D_ + i + 2 * j);
      a += p[0]; b += p[1];
    }
    o[j] = pack2(bflo(xv[j]) + gb[2 * j] * a, bfhi(xv[j]) + gb[2 * j + 1] * b);
  }
  *reinterpret_cast<u32x4*>(out + i) = o;
}

extern "C" void kernel_launch(void* const* d_in, const int* in_sizes, int n_in,
                              void* d_out, int out_size, void* d_ws, size_t ws_size,
                              hipStream_t stream) {
  const u16* x = (const u16*)d_in[0];
  const u16* wqkv = (const u16*)d_in[1];
  const u16* wo = (const u16*)d_in[2];
  const u16* wg = (const u16*)d_in[4];
  const u16* wu = (const u16*)d_in[5];
  const u16* wd = (const u16*)d_in[6];
  const u16* c = (const u16*)d_in[7];
  const u16* wmod = (const u16*)d_in[8];
  char* ws = (char*)d_ws;
  float* mod = (float*)(ws + WS_MOD);
  float2* st1 = (float2*)(ws + WS_ST1);
  float2* st2 = (float2*)(ws + WS_ST2);
  u16* Q = (u16*)(ws + WS_Q);
  u16* K = (u16*)(ws + WS_K);
  u16* V = (u16*)(ws + WS_V);
  u16* O = (u16*)(ws + WS_O);
  u16* x1 = (u16*)(ws + WS_X1);
  u16* h2 = (u16*)(ws + WS_H2);
  u16* hh = (u16*)(ws + WS_HH);
  float* part = (float*)(ws + WS_PART);

  (void)hipFuncSetAttribute((const void*)k_qkv, hipFuncAttributeMaxDynamicSharedMemorySize, LDS_QKV);
  (void)hipFuncSetAttribute((const void*)k_proj, hipFuncAttributeMaxDynamicSharedMemorySize, LDS_PROJ);
  (void)hipFuncSetAttribute((const void*)k_glu, hipFuncAttributeMaxDynamicSharedMemorySize, LDS_GLU);
  (void)hipFuncSetAttribute((const void*)k_down, hipFuncAttributeMaxDynamicSharedMemorySize, LDS_DOWN);

  k_prep<<<108 + 512, 256, 0, stream>>>(c, wmod, x, mod, st1);
  k_qkv<<<192, 512, LDS_QKV, stream>>>(x, wqkv, st1, mod, Q);
  k_attn<<<32 * 16, 512, 0, stream>>>(Q, K, V, O);
  k_proj<<<256, 192, LDS_PROJ, stream>>>(O, wo, x, mod, x1, st2);
  k_ln2<<<512, 256, 0, stream>>>(x1, st2, mod, h2);
  k_glu<<<256, 512, LDS_GLU, stream>>>(h2, wg, wu, hh);
  k_down<<<256, 512, LDS_DOWN, stream>>>(hh, wd, part);
  k_final<<<(S_ * D_ / 8 + 255) / 256, 256, 0, stream>>>(part, x1, mod, (u16*)d_out);
}
```

```cpp
#include <hip/hip_runtime.h>
#include <hip/hip_bf16.h>
#include <stdint.h>
#include <type_traits>

typedef __attribute__((ext_vector_type(8))) short bf16x8;
typedef __attribute__((ext_vector_type(4))) short s16x4;
typedef __attribute__((ext_vector_type(4))) float f32x4;
typedef __attribute__((ext_vector_type(2))) float f32x2;
typedef __attribute__((ext_vector_type(16))) float f32x16;
typedef __attribute__((ext_vector_type(4))) unsigned u32x4;
typedef __attribute__((ext_vector_type(2))) unsigned u32x2;
typedef __attribute__((ext_vector_type(2))) __bf16 bf16x2_t;
typedef unsigned short u16;

#define DI __device__ __forceinline__

constexpr int S_ = 2048, D_ = 576, H_ = 32, DH_ = 18, F_ = 4608;
constexpr int HP = 32;
constexpr float QSCALE = 0.23570226039551584f * 1.4426950408889634f;

DI float bf2f(u16 v) { return __uint_as_float(((unsigned)v) << 16); }
DI float bflo(unsigned v) { return __uint_as_float(v << 16); }
DI float bfhi(unsigned v) { return __uint_as_float(v & 0xffff0000u); }
DI unsigned pack2(float a, float b) {
  f32x2 v = {a, b};
  bf16x2_t r = __builtin_convertvector(v, bf16x2_t);
  return __builtin_bit_cast(unsigned, r);
}
DI u16 f2bf(float a) { return (u16)(pack2(a, 0.f) & 0xffffu); }
DI float rbf(float a) { return bflo(pack2(a, 0.f) & 0xffffu); }

typedef __attribute__((address_space(3))) s16x4* lds_s16x4_ptr;
DI s16x4 tr_read(const char* p) {
  return __builtin_amdgcn_ds_read_tr16_b64_v4i16((lds_s16x4_ptr)(p));
}

constexpr size_t WS_MOD = 0;
constexpr size_t WS_ST1 = 16384;
constexpr size_t WS_ST2 = WS_ST1 + 2048 * 8;
constexpr size_t WS_Q = 1 << 20;
constexpr size_t WS_K = WS_Q + (size_t)H_ * S_ * HP * 2;
constexpr size_t WS_V = WS_K + (size_t)H_ * S_ * HP * 2;
constexpr size_t WS_O = WS_V + (size_t)H_ * S_ * HP * 2;
constexpr size_t WS_X1 = WS_O + (size_t)S_ * D_ * 2;
constexpr size_t WS_H2 = WS_X1 + (size_t)S_ * D_ * 2;
constexpr size_t WS_HH = WS_H2 + (size_t)S_ * D_ * 2;
constexpr size_t WS_PART = WS_HH + (size_t)S_ * F_ * 2;

__global__ __launch_bounds__(256) void k_prep(const u16* __restrict__ c, const u16* __restrict__ wmod,
                                               const u16* __restrict__ x, float* __restrict__ mod,
                                               float2* __restrict__ st1) {
  __shared__ float red[64][33];
  const int t = threadIdx.x;
  if (blockIdx.x < 108) {
    const int n0 = blockIdx.x * 32 + (t & 3) * 8;
    const int ks = t >> 2;
    float acc[8];
#pragma unroll
    for (int j = 0; j < 8; ++j) acc[j] = 0.f;
#pragma unroll
    for (int r = 0; r < 9; ++r) {
      const int k = ks * 9 + r;
      float cv = bf2f(c[k]);
      float sv = rbf(cv / (1.f + __expf(-cv)));
      u32x4 w = *reinterpret_cast<const u32x4*>(wmod + (size_t)k * 3456 + n0);
#pragma unroll
      for (int j = 0; j < 4; ++j) {
        acc[2 * j] += sv * bflo(w[j]);
        acc[2 * j + 1] += sv * bfhi(w[j]);
      }
    }
#pragma unroll
    for (int j = 0; j < 8; ++j) red[ks][(t & 3) * 8 + j] = acc[j];
    __syncthreads();
    if (t < 32) {
      float s = 0.f;
#pragma unroll 8
      for (int i = 0; i < 64; ++i) s += red[i][t];
      mod[blockIdx.x * 32 + t] = rbf(s);
    }
  } else {
    const int lane = t & 63, w = t >> 6;
    for (int row = (blockIdx.x - 108) * 4 + w; row < S_; row += (gridDim.x - 108) * 4) {
      const u16* xr = x + (size_t)row * D_;
      u32x4 a = *reinterpret_cast<const u32x4*>(xr + lane * 8);
      u32x4 b = {0, 0, 0, 0};
      if (lane < 8) b = *reinterpret_cast<const u32x4*>(xr + 512 + lane * 8);
      float v[16];
#pragma unroll
      for (int j = 0; j < 4; ++j) {
        v[2 * j] = bflo(a[j]); v[2 * j + 1] = bfhi(a[j]);
        v[8 + 2 * j] = bflo(b[j]); v[8 + 2 * j + 1] = bfhi(b[j]);
      }
      float s = 0.f;
#pragma unroll
      for (int j = 0; j < 16; ++j) s += v[j];
#pragma unroll
      for (int o = 32; o >= 1; o >>= 1) s += __shfl_xor(s, o);
      const float mean = s * (1.f / D_);
      float q = 0.f;
#pragma unroll
      for (int j = 0; j < 8; ++j) { float d = v[j] - mean; q += d * d; }
      if (lane < 8) {
#pragma unroll
        for (int j = 8; j < 16; ++j) { float d = v[j] - mean; q += d * d; }
      }
#pragma unroll
      for (int o = 32; o >= 1; o >>= 1) q += __shfl_xor(q, o);
      if (lane == 0) st1[row] = make_float2(mean, rsqrtf(q * (1.f / D_) + 1e-5f));
    }
  }
}

#define WAIT_LGKM0() asm volatile("s_waitcnt lgkmcnt(0)" ::: "memory")
#define STAMP(i)
#define RAW_BAR() __builtin_amdgcn_s_barrier()
template <int N> DI void wait_vm() { asm volatile("s_waitcnt vmcnt(%0)" ::"n"(N) : "memory"); }
template <int OFF> DI void lds_tr(u32x2& d, unsigned a) { asm volatile("ds_read_b64_tr_b16 %0, %1 offset:%2" : "=v"(d) : "v"(a), "n"(OFF)); }
template <int OFF> DI void lds_128(u32x4& d, unsigned a) { asm volatile("ds_read_b128 %0, %1 offset:%2" : "=v"(d) : "v"(a), "n"(OFF)); }
DI void pin4(u32x4& a) { asm volatile("" : "+v"(a)); }
template <int N> DI void wait_l2(u32x2& a, u32x2& b) { asm volatile("s_waitcnt lgkmcnt(%2)" : "+v"(a), "+v"(b) : "n"(N)); }
template <int N> DI void wait_l4(u32x2& a, u32x2& b, u32x4& c, u32x4& d) { asm volatile("s_waitcnt lgkmcnt(%4)" : "+v"(a), "+v"(b), "+v"(c), "+v"(d) : "n"(N)); }

template <int BK> DI int a_swz(int row) { return BK >= 64 ? ((row >> 1) & 7) : ((-(row >> 2)) & 3); }

template <int I, int N, class F> DI void static_for(F&& f) {
  if constexpr (I < N) { f(std::integral_constant<int, I>{}); static_for<I + 1, N>(f); }
}

template <int BM, int BN, int NIMG, int MT, int WM, int WN, int BK, int NST, int LNMODE  , class Epi>
DI void gemm_body(char* lds, const u16* __restrict__ A, int lda, int m0, int kbeg, int nk,
                  const float2* __restrict__ stats, const float* __restrict__ mscale, const float* __restrict__ mshift,
                  const u16* __restrict__ B0, const u16* __restrict__ B1, int ldb, Epi& epi) {
  constexpr int NW = WM * WN, NTHR = 64 * NW;
  constexpr int NT = BN / 16, NTW0 = (NT + WN - 1) / WN, NTWL = NT - (WN - 1) * NTW0, BNI = BN / NIMG, NTI = NT / NIMG;
  constexpr int ABYTES = BM * BK * 2, BBYTES = BK * BN * 2, STAGE = ABYTES + BBYTES;
  constexpr int AINS = ABYTES / 1024, BINS = BBYTES / 1024;
  constexpr int BINS_I = BINS / NIMG;
  constexpr int TINS = AINS + BINS;
  constexpr int G = (TINS + NW - 1) / NW;
  constexpr int SUB = BK / 32;
  constexpr int CPR = BK >= 64 ? 8 : 4;
  constexpr int ARB = CPR * 16;
  static_assert(BBYTES % (1024 * NIMG) == 0 && ABYTES % 1024 == 0 && BM == WM * MT * 16, "tile");
  static_assert(NIMG == 1 || NTW0 == NTI, "img");
  const int t = threadIdx.x, lane = t & 63;
  const int wid = __builtin_amdgcn_readfirstlane(t >> 6);
  const int wr = wid % WM, wn = wid / WM, fr = lane & 15, fq = lane >> 4;
  float* lnp = reinterpret_cast<float*>(lds + NST * STAGE);

  STAMP(0);
  unsigned goff[G];
#pragma unroll
  for (int ii = 0; ii < G; ++ii) {
    int i = wid + ii * NW;
    if (i >= TINS) i -= TINS;
    if (i < AINS) {
      const int j = i * 64 + lane;
      const int si = j / (BM * CPR), row = (j / CPR) % BM, pos = j % CPR;
      goff[ii] = (unsigned)((row * lda + si * 64 + (pos ^ a_swz<BK>(row)) * 8) * 2);
    } else {
      const int j = ((i - AINS) % BINS_I) * 64 + lane;
      const int kb = j / BNI, rem = j % BNI;
      const int sub = rem >> 4, r = (rem >> 1) & 7, hf = rem & 1;
      goff[ii] = (unsigned)(((kb * 8 + (r ^ ((kb & 1) << 2))) * ldb + sub * 16 + hf * 8) * 2);
    }
  }
  const char* Abase = (const char*)(A + (size_t)m0 * lda + kbeg);
  const char* B0base = (const char*)(B0 + (size_t)kbeg * ldb);
  const char* B1base = (const char*)(B1 + (size_t)kbeg * ldb);

  auto issue_one = [&](int kt, int ii) {
    char* buf = lds + (kt % NST) * STAGE;
    int i = wid + ii * NW;
    if (i >= TINS) i -= TINS;
    const char* base = (i < AINS) ? (Abase + kt * BK * 2)
                                  : (((NIMG == 2 && i - AINS >= BINS_I) ? B1base : B0base) + (size_t)kt * BK * ldb * 2);
    __builtin_amdgcn_global_load_lds((const unsigned*)(base + goff[ii]),
                                     (__attribute__((address_space(3))) unsigned*)(buf + (i * 64 + lane) * 16), 16, 0, 0);
  };

#pragma unroll
  for (int st = 0; st < NST - 2; ++st)
    if (st < nk) {
#pragma unroll
      for (int ii = 0; ii < G; ++ii) issue_one(st, ii);
    }

  float ra[MT], rb[MT];
  if constexpr (LNMODE != 0) {
    for (int i = t; i < D_; i += NTHR) { lnp[i] = mscale[i] + 1.f; lnp[D_ + i] = mshift[i]; }
#pragma unroll
    for (int m = 0; m < MT; ++m) {
      float2 s = stats[m0 + (wr * MT + m) * 16 + fr];
      ra[m] = s.y; rb[m] = -s.x * s.y;
    }
    wait_vm<0>();
    WAIT_LGKM0();
  }

  f32x4 acc[MT][NTW0];
#pragma unroll
  for (int m = 0; m < MT; ++m)
#pragma unroll
    for (int n = 0; n < NTW0; ++n) acc[m][n] = f32x4{0.f, 0.f, 0.f, 0.f};

  const unsigned lbase = (unsigned)(size_t)lds;
  const int q4 = fr >> 2, p4 = lane & 3, sw = (fq & 1) << 2;
  const int arow = wr * MT * 16 + fr;
  const unsigned aoffb = arow * ARB;
  const int tile0 = wn * NTW0;
  const unsigned bimg = (unsigned)((tile0 / NTI) * (BBYTES / NIMG) + (tile0 % NTI) * 256);
  const unsigned boff_lo = ABYTES + bimg + fq * (BNI * 16) + ((q4 ^ sw) << 5) + p4 * 8;
  const unsigned boff_hi = ABYTES + bimg + fq * (BNI * 16) + (((q4 + 4) ^ sw) << 5) + p4 * 8;

  auto mainloop = [&](auto NTWC) {
    constexpr int NTW = decltype(NTWC)::value;
    constexpr int T = SUB * NTW;
    constexpr int PF = (T % 3 == 0) ? 3 : 2;
    u32x2 blo[PF], bhi[PF];
    u32x4 af[2][MT];
    auto rda = [&](unsigned sbx, auto S2, auto PAR) {
      constexpr int s2 = decltype(S2)::value, par = decltype(PAR)::value;
      const int ch = (s2 & (CPR / 4 - 1)) * 4 + fq;
      const unsigned base = sbx + (s2 / (CPR / 4)) * (BM * ARB) + aoffb;
      lds_128<0>(af[par][0], base + ((ch ^ a_swz<BK>(arow)) << 4));
      if constexpr (MT > 1) lds_128<16 * ARB>(af[par][MT > 1 ? 1 : 0], base + ((ch ^ a_swz<BK>(arow + 16)) << 4));
      if constexpr (MT > 2) {
        lds_128<32 * ARB>(af[par][MT > 2 ? 2 : 0], base + ((ch ^ a_swz<BK>(arow + 32)) << 4));
        lds_128<48 * ARB>(af[par][MT > 2 ? 3 : 0], base + ((ch ^ a_swz<BK>(arow + 48)) << 4));
      }
    };
    auto rdb = [&](unsigned sbx, auto IDX) {
      constexpr int idx = decltype(IDX)::value;
      constexpr int s2 = idx / NTW, n = idx % NTW;
      constexpr int off = s2 * 4 * (BNI * 16) + n * 256;
      lds_tr<off>(blo[idx % PF], sbx + boff_lo);
      lds_tr<off>(bhi[idx % PF], sbx + boff_hi);
    };
    if (NST - 3 < nk - 1) wait_vm<G * (NST - 3)>(); else wait_vm<0>();
    RAW_BAR();
    rda(lbase, std::integral_constant<int, 0>{}, std::integral_constant<int, 0>{});
    static_for<0, PF - 1>([&](auto I) { rdb(lbase, I); });

    auto stage = [&](int kt, auto PAR0, auto LAST_) {
      constexpr int par0 = decltype(PAR0)::value;
      constexpr bool LAST = decltype(LAST_)::value;
      STAMP(2 + 2 * kt);
      const bool more = kt + NST - 2 < nk;
      const unsigned sb = lbase + (kt % NST) * STAGE;
      const unsigned sbn = lbase + ((kt + 1) % NST) * STAGE;
      static_for<0, T>([&](auto IDX) {
        constexpr int idx = decltype(IDX)::value;
        constexpr int s2 = idx / NTW, n = idx % NTW;
        constexpr int par = (par0 + s2) & 1;
        if constexpr (!LAST && idx == T - PF) {
          if (kt + 1 + (NST - 3) < nk) wait_vm<G * (NST - 3)>(); else wait_vm<0>();
          RAW_BAR();
        }
        constexpr bool rdA = (n == NTW - PF) && (s2 + 1 < SUB || !LAST);
        if constexpr (rdA) {
          if constexpr (s2 + 1 < SUB) rda(sb, std::integral_constant<int, s2 + 1>{}, std::integral_constant<int, par ^ 1>{});
          else rda(sbn, std::integral_constant<int, 0>{}, std::integral_constant<int, par ^ 1>{});
        }
        constexpr bool rdB = (idx + PF - 1 < T) || !LAST;
        if constexpr (rdB) {
          if constexpr (idx + PF - 1 < T) rdb(sb, std::integral_constant<int, idx + PF - 1>{});
          else rdb(sbn, std::integral_constant<int, idx + PF - 1 - T>{});
        }
        constexpr int nb = LAST ? (((idx + PF - 1 < T) ? (idx + PF - 1) : (T - 1)) - idx) : (PF - 1);
        constexpr int nA = (n + PF) / NTW - 1 + 0;
        constexpr bool thisA = (n >= NTW - PF) && (n <= NTW - 2) && (s2 + 1 < SUB || !LAST);
        constexpr int cnt = 2 * nb + (thisA ? MT : 0);
        (void)nA;
        if constexpr (n == 0) {
          asm volatile("s_waitcnt lgkmcnt(%0)" ::"n"(cnt));
          pin4(af[par][0]);
          if constexpr (MT > 1) pin4(af[par][MT > 1 ? 1 : 0]);
          if constexpr (MT > 2) { pin4(af[par][MT - 2]); pin4(af[par][MT - 1]); }
          asm volatile("" : "+v"(blo[idx % PF]), "+v"(bhi[idx % PF]));
          if constexpr (LNMODE != 0) {
            const int kc = kbeg + kt * BK + s2 * 32 + fq * 8;
            f32x4 sc0 = *reinterpret_cast<const f32x4*>(lnp + kc), sc1 = *reinterpret_cast<const f32x4*>(lnp + kc + 4);
            f32x4 sh0 = *reinterpret_cast<const f32x4*>(lnp + D_ + kc), sh1 = *reinterpret_cast<const f32x4*>(lnp + D_ + kc + 4);
#pragma unroll
            for (int m = 0; m < MT; ++m) {
              u32x4 r = af[par][m], o;
              const float a = ra[m], b = rb[m];
              o[0] = pack2((bflo(r[0]) * a + b) * sc0[0] + sh0[0], (bfhi(r[0]) * a + b) * sc0[1] + sh0[1]);
              o[1] = pack2((bflo(r[1]) * a + b) * sc0[2] + sh0[2], (bfhi(r[1]) * a + b) * sc0[3] + sh0[3]);
              o[2] = pack2((bflo(r[2]) * a + b) * sc1[0] + sh1[0], (bfhi(r[2]) * a + b) * sc1[1] + sh1[1]);
              o[3] = pack2((bflo(r[3]) * a + b) * sc1[2] + sh1[2], (bfhi(r[3]) * a + b) * sc1[3] + sh1[3]);
              af[par][m] = o;
            }
          }
        } else {
          wait_l2<cnt>(blo[idx % PF], bhi[idx % PF]);
        }
        u32x4 bw = {blo[idx % PF][0], blo[idx % PF][1], bhi[idx % PF][0], bhi[idx % PF][1]};
        const bf16x8 bf = __builtin_bit_cast(bf16x8, bw);
#pragma unroll
        for (int m = 0; m < MT; ++m)
          acc[m][n] = __builtin_amdgcn_mfma_f32_16x16x32_bf16(bf, __builtin_bit_cast(bf16x8, af[par][m]), acc[m][n], 0, 0, 0);
        if constexpr (!LAST) {
          static_for<0, G>([&](auto II) {
            constexpr int ii = decltype(II)::value;
            if constexpr (idx == (ii * (T - PF)) / G) { if (more) issue_one(kt + NST - 2, ii); }
          });
        }
      });
    };
    if constexpr (SUB % 2 == 0) {
      for (int kt = 0; kt < nk - 1; ++kt) stage(kt, std::integral_constant<int, 0>{}, std::false_type{});
      stage(nk - 1, std::integral_constant<int, 0>{}, std::true_type{});
    } else {
      for (int kt = 0; kt < nk - 2; kt += 2) {
        stage(kt, std::integral_constant<int, 0>{}, std::false_type{});
        stage(kt + 1, std::integral_constant<int, 1>{}, std::false_type{});
      }
      stage(nk - 2, std::integral_constant<int, 0>{}, std::false_type{});
      stage(nk - 1, std::integral_constant<int, 1>{}, std::true_type{});
    }
  };
  if constexpr (NTWL != NTW0) {
    if (wn == WN - 1) mainloop(std::integral_constant<int, NTWL>{});
    else mainloop(std::integral_constant<int, NTW0>{});
  } else {
    mainloop(std::integral_constant<int, NTW0>{});
  }
  STAMP(50);
  __syncthreads();
  STAMP(51);
  const int ntw = (wn == WN - 1) ? NTWL : NTW0;
  epi.template run<MT, NTW0, NTHR>(acc, lds, wr * MT * 16 + fr, tile0, ntw, fq);
  STAMP(52);
}

struct EpiQKV {
  u16* qkv;
  int m0, n0;
  template <int MT, int NTW, int NTHR>
  DI void run(f32x4 (&acc)[MT][NTW], char* lds, int row0, int tile0, int ntw, int fq) const {
    constexpr int RS = 296;
    const int which = n0 / D_;
    const float sc = which == 0 ? QSCALE : 1.f;
#pragma unroll
    for (int m = 0; m < MT; ++m)
#pragma unroll
      for (int n = 0; n < NTW; ++n) {
        if (n < ntw) {
          u32x2 o;
          o[0] = pack2(acc[m][n][0] * sc, acc[m][n][1] * sc);
          o[1] = pack2(acc[m][n][2] * sc, acc[m][n][3] * sc);
          *reinterpret_cast<u32x2*>(lds + (row0 + m * 16) * RS + ((tile0 + n) * 16 + fq * 4) * 2) = o;
        }
      }
    __syncthreads();
    const int head0 = (n0 % D_) / DH_;
    const unsigned padw = (which == 2) ? 0x00003f80u : 0u;
    for (int it = threadIdx.x; it < 8 * 128 * 4; it += NTHR) {
      const int ch = it & 3, row = (it >> 2) & 127, hd = it >> 9;
      const unsigned* src = reinterpret_cast<const unsigned*>(lds + row * RS + hd * 36 + ch * 16);
      u32x4 w = {0u, 0u, 0u, 0u};
      if (ch < 2) w = u32x4{src[0], src[1], src[2], src[3]};
      else if (ch == 2) w = u32x4{src[0], padw, 0u, 0u};
      *reinterpret_cast<u32x4*>(qkv + ((size_t)(which * H_ + head0 + hd) * S_ + m0 + row) * HP + ch * 8) = w;
    }
  }
};

constexpr int LDS_QKV = 4 * (128 * 64 * 2 + 64 * 144 * 2) + 2 * D_ * 4;
__global__ __launch_bounds__(512) void k_qkv(const u16* __restrict__ x, const u16* __restrict__ wqkv,
                                              const float2* __restrict__ st1, const float* __restrict__ mod,
                                              u16* __restrict__ qkv) {
  extern __shared__ __attribute__((aligned(16))) char lds[];
  const int b = blockIdx.x;
  const int xcd = b & 7, idx = b >> 3;
  const int bn = (idx % 3) * 4 + (xcd >> 1), bm = (idx / 3) * 2 + (xcd & 1);
  const int n0 = bn * 144;
  EpiQKV epi{qkv, bm * 128, n0};
  gemm_body<128, 144, 1, 2, 4, 2, 64, 4, 1>(lds, x, D_, bm * 128, 0, D_ / 64, st1, mod + D_, mod, wqkv + n0, wqkv + n0, 3 * D_, epi);
}

__global__ __launch_bounds__(512, 4) void k_attn(const u16* __restrict__ Q, const u16* __restrict__ K,
                                                  const u16* __restrict__ V, u16* __restrict__ O) {
  __shared__ __attribute__((aligned(16))) char lds_all[2 * 3 * 8192];
  const int t = threadIdx.x, lane = t & 63, wid = t >> 6, r32 = lane & 31, h = lane >> 5;
  const int half = wid >> 2, tl = t & 255;
  char* lds = lds_all + half * (3 * 8192);
  const int head = blockIdx.x >> 4, qb = blockIdx.x & 15;
  const int q0 = qb * 128 + (wid & 3) * 32;
  const u16* Qh = Q + (size_t)head * S_ * HP;
  const u16* Kh = K + ((size_t)head * S_ + half * 1024) * HP;
  const u16* Vh = V + ((size_t)head * S_ + half * 1024) * HP;
  const bf16x8 qf0 = *reinterpret_cast<const bf16x8*>(Qh + (size_t)(q0 + r32) * HP + 8 * h);
  const bf16x8 qf1 = *reinterpret_cast<const bf16x8*>(Qh + (size_t)(q0 + r32) * HP + 16 + 8 * h);

  const int skey = tl >> 2, sch = tl & 3;
  const int kst_off = skey * 64 + ((sch ^ ((skey >> 2) & 3)) << 4);
  const int vst_off = 4096 + skey * 64 + sch * 16;
  u32x4 kreg, vreg;
  auto gload = [&](int st) {
    kreg = *reinterpret_cast<const u32x4*>(Kh + (size_t)(st * 64 + skey) * HP + sch * 8);
    vreg = *reinterpret_cast<const u32x4*>(Vh + (size_t)(st * 64 + skey) * HP + sch * 8);
  };
  auto swrite = [&](char* buf) {
    *reinterpret_cast<u32x4*>(buf + kst_off) = kreg;
    *reinterpret_cast<u32x4*>(buf + vst_off) = vreg;
  };

  f32x16 oacc, negm;
  constexpr float THR = 40.f;

  const int kr0 = r32 * 64 + (((0 + h) ^ ((r32 >> 2) & 3)) << 4);
  const int kr1 = r32 * 64 + (((2 + h) ^ ((r32 >> 2) & 3)) << 4);
  const int g = lane >> 4, q4 = (lane & 15) >> 2, p4 = lane & 3;
  const int vr = 4096 + (4 * (g >> 1) + q4) * 64 + (g & 1) * 32 + p4 * 8;
  constexpr int NS = 1024 / 64;

  auto run = [&](auto SAFE_) {
    constexpr bool SAFE = decltype(SAFE_)::value;
#pragma unroll
    for (int i = 0; i < 16; ++i) { oacc[i] = 0.f; negm[i] = 0.f; }
    gload(0); swrite(lds);
    gload(1); swrite(lds + 8192);
    __syncthreads();
    auto qk = [&](const char* buf, int u) -> f32x16 {
      const bf16x8 ka = *reinterpret_cast<const bf16x8*>(buf + u * 2048 + kr0);
      const bf16x8 kb = *reinterpret_cast<const bf16x8*>(buf + u * 2048 + kr1);
      f32x16 s = __builtin_amdgcn_mfma_f32_32x32x16_bf16(ka, qf0, negm, 0, 0, 0);
      return __builtin_amdgcn_mfma_f32_32x32x16_bf16(kb, qf1, s, 0, 0, 0);
    };
    f32x16 sc = qk(lds, 0);
    {
      float mx = fmaxf(sc[0], sc[1]);
#pragma unroll
      for (int i = 2; i < 16; ++i) mx = fmaxf(mx, sc[i]);
      mx = fmaxf(mx, __shfl_xor(mx, 32));
#pragma unroll
      for (int i = 0; i < 16; ++i) { negm[i] = -mx; sc[i] -= mx; }
    }
    int bcur = 0;
    for (int st = 0; st < NS; ++st) {
      const char* cur = lds + bcur * 8192;
      const int bn1 = bcur == 2 ? 0 : bcur + 1, bn2 = bn1 == 2 ? 0 : bn1 + 1;
      const char* nx1 = lds + bn1 * 8192;
      if (st + 2 < NS) gload(st + 2);
#pragma unroll
      for (int u = 0; u < 2; ++u) {
        f32x16 sn;
        if (u < 1) sn = qk(cur, u + 1);
        else sn = qk(nx1, 0);
        if constexpr (SAFE) {
          float mx = fmaxf(sc[0], sc[1]);
#pragma unroll
          for (int i = 2; i < 16; ++i) mx = fmaxf(mx, sc[i]);
          if (!__all(mx <= THR)) {
            mx = fmaxf(mx, __shfl_xor(mx, 32));
            const float d = fmaxf(mx, 0.f);
            const float alpha = __builtin_amdgcn_exp2f(-d);
#pragma unroll
            for (int i = 0; i < 16; ++i) { oacc[i] *= alpha; negm[i] -= d; sc[i] -= d; sn[i] -= d; }
          }
        }
        u32x4 pf0, pf1;
#pragma unroll
        for (int i = 0; i < 4; ++i) {
          pf0[i] = pack2(__builtin_amdgcn_exp2f(sc[2 * i]), __builtin_amdgcn_exp2f(sc[2 * i + 1]));
          pf1[i] = pack2(__builtin_amdgcn_exp2f(sc[8 + 2 * i]), __builtin_amdgcn_exp2f(sc[8 + 2 * i + 1]));
        }
        {
          s16x4 lo = tr_read(cur + vr + (32 * u) * 64);
          s16x4 hi = tr_read(cur + vr + (32 * u + 8) * 64);
          s16x4 lo2 = tr_read(cur + vr + (32 * u + 16) * 64);
          s16x4 hi2 = tr_read(cur + vr + (32 * u + 24) * 64);
          const bf16x8 vf0 = __builtin_shufflevector(lo, hi, 0, 1, 2, 3, 4, 5, 6, 7);
          const bf16x8 vf1 = __builtin_shufflevector(lo2, hi2, 0, 1, 2, 3, 4, 5, 6, 7);
          oacc = __builtin_amdgcn_mfma_f32_32x32x16_bf16(vf0, __builtin_bit_cast(bf16x8, pf0), oacc, 0, 0, 0);
          oacc = __builtin_amdgcn_mfma_f32_32x32x16_bf16(vf1, __builtin_bit_cast(bf16x8, pf1), oacc, 0, 0, 0);
        }
        sc = sn;
      }
      if (st + 2 < NS) swrite(lds + bn2 * 8192);
      __syncthreads();
      bcur = bn1;
    }
  };
  run(std::false_type{});
  {
    const float l = oacc[10];
    const bool bad = (h == 0) && !(l < 1.0e30f);
    if (__syncthreads_or(bad)) run(std::true_type{});
  }
  float* xch = reinterpret_cast<float*>(lds_all);
  if (half == 1) {
#pragma unroll
    for (int i = 0; i < 11; ++i) xch[(i * 4 + (wid & 3)) * 64 + lane] = oacc[i];
    xch[(11 * 4 + (wid & 3)) * 64 + lane] = negm[0];
  }
  __syncthreads();
  if (half == 1) return;
  {
    const float nm1 = xch[(11 * 4 + wid) * 64 + lane], nm0 = negm[0];
    const float nm = fminf(nm0, nm1);
    const float f0 = __builtin_amdgcn_exp2f(nm - nm0), f1 = __builtin_amdgcn_exp2f(nm - nm1);
#pragma unroll
    for (int i = 0; i < 11; ++i) oacc[i] = oacc[i] * f0 + xch[(i * 4 + wid) * 64 + lane] * f1;
  }
  const float l = __shfl(oacc[10], r32);
  const float inv = 1.f / l;
  unsigned* op = reinterpret_cast<unsigned*>(O + (size_t)(q0 + r32) * D_ + head * DH_);
  const unsigned w0 = pack2(oacc[0] * inv, oacc[1] * inv), w1 = pack2(oacc[2] * inv, oacc[3] * inv);
  const unsigned w2 = pack2(oacc[4] * inv, oacc[5] * inv), w3 = pack2(oacc[6] * inv, oacc[7] * inv);
  const unsigned w4 = pack2(oacc[8] * inv, oacc[9] * inv);
  op[2 * h] = w0; op[2 * h + 1] = w1; op[4 + 2 * h] = w2; op[5 + 2 * h] = w3;
  if (h == 0) op[8] = w4;
}

struct EpiProj {
  const u16* x; const float* ga; u16* x1; float2* st2; int m0, n0;
  template <int MT, int NTW, int NTHR>
  DI void run(f32x4 (&acc)[MT][NTW], char* lds, int row0, int tile0, int ntw, int fq) const {
    const int part = (n0 / 144) * 4 + tile0 / NTW;
#pragma unroll
    for (int m = 0; m < MT; ++m) {
      const int row = m0 + row0 + m * 16;
      float s = 0.f;
#pragma unroll
      for (int n = 0; n < NTW; ++n) {
        if (n < ntw) {
          const int col = n0 + (tile0 + n) * 16 + fq * 4;
          u32x2 xv = *reinterpret_cast<const u32x2*>(x + (size_t)row * D_ + col);
          f32x4 g = *reinterpret_cast<const f32x4*>(ga + col);
          u32x2 o;
          o[0] = pack2(bflo(xv[0]) + g[0] * acc[m][n][0], bfhi(xv[0]) + g[1] * acc[m][n][1]);
          o[1] = pack2(bflo(xv[1]) + g[2] * acc[m][n][2], bfhi(xv[1]) + g[3] * acc[m][n][3]);
          *reinterpret_cast<u32x2*>(x1 + (size_t)row * D_ + col) = o;
          acc[m][n] = f32x4{bflo(o[0]), bfhi(o[0]), bflo(o[1]), bfhi(o[1])};
          s += (acc[m][n][0] + acc[m][n][1]) + (acc[m][n][2] + acc[m][n][3]);
        }
      }
      s += __shfl_xor(s, 16); s += __shfl_xor(s, 32);
      const float lm = s / (float)(16 * ntw);
      float q = 0.f;
#pragma unroll
      for (int n = 0; n < NTW; ++n)
        if (n < ntw) {
#pragma unroll
          for (int j = 0; j < 4; ++j) { float d = acc[m][n][j] - lm; q += d * d; }
        }
      q += __shfl_xor(q, 16); q += __shfl_xor(q, 32);
      if (fq == 0) st2[(size_t)row * 16 + part] = make_float2(s, q);
    }
  }
};

constexpr int LDS_PROJ = 4 * (32 * 64 * 2 + 64 * 144 * 2);
__global__ __launch_bounds__(192) void k_proj(const u16* __restrict__ o, const u16* __restrict__ wo,
                                               const u16* __restrict__ x, const float* __restrict__ mod,
                                               u16* __restrict__ x1, float2* __restrict__ st2) {
  extern __shared__ __attribute__((aligned(16))) char lds[];
  const int b = blockIdx.x;
  const int bn = b & 3, bm = b >> 2;
  const int n0 = bn * 144;
  EpiProj epi{x, mod + 2 * D_, x1, st2, bm * 32, n0};
  gemm_body<32, 144, 1, 2, 1, 3, 64, 4, 0>(lds, o, D_, bm * 32, 0, D_ / 64, nullptr, nullptr, nullptr, wo + n0, wo + n0, D_, epi);
}

__global__ __launch_bounds__(256) void k_ln2(const u16* __restrict__ x1, const float2* __restrict__ st2,
                                              const float* __restrict__ mod, u16* __restrict__ h2) {
  const int row = blockIdx.x * 4 + (threadIdx.x >> 6), lane = threadIdx.x & 63;
  const float2 pp = st2[(size_t)row * 16 + (lane & 15)];
  const float cnt = ((lane & 3) == 3) ? 0.f : 48.f;
  float s = (cnt > 0.f) ? pp.x : 0.f;
#pragma unroll
  for (int o = 8; o >= 1; o >>= 1) s += __shfl_xor(s, o);
  const float mean = s * (1.f / D_);
  float d = (cnt > 0.f) ? (pp.x / 48.f - mean) : 0.f;
  float m2 = (cnt > 0.f) ? (pp.y + cnt * d * d) : 0.f;
#pragma unroll
  for (int o = 8; o >= 1; o >>= 1) m2 += __shfl_xor(m2, o);
  const float a = rsqrtf(m2 * (1.f / D_) + 1e-5f), b = -mean * a;
  const float* sb = mod + 3 * D_;
  const float* cb = mod + 4 * D_;
  for (int c = lane; c < 72; c += 64) {
    u32x4 r = *reinterpret_cast<const u32x4*>(x1 + (size_t)row * D_ + c * 8);
    f32x4 sc0 = *reinterpret_cast<const f32x4*>(cb + c * 8), sc1 = *reinterpret_cast<const f32x4*>(cb + c * 8 + 4);
    f32x4 sh0 = *reinterpret_cast<const f32x4*>(sb + c * 8), sh1 = *reinterpret_cast<const f32x4*>(sb + c * 8 + 4);
    u32x4 o;
    o[0] = pack2((bflo(r[0]) * a + b) * (sc0[0] + 1.f) + sh0[0], (bfhi(r[0]) * a + b) * (sc0[1] + 1.f) + sh0[1]);
    o[1] = pack2((bflo(r[1]) * a + b) * (sc0[2] + 1.f) + sh0[2], (bfhi(r[1]) * a + b) * (sc0[3] + 1.f) + sh0[3]);
    o[2] = pack2((bflo(r[2]) * a + b) * (sc1[0] + 1.f) + sh1[0], (bfhi(r[2]) * a + b) * (sc1[1] + 1.f) + sh1[1]);
    o[3] = pack2((bflo(r[3]) * a + b) * (sc1[2] + 1.f) + sh1[2], (bfhi(r[3]) * a + b) * (sc1[3] + 1.f) + sh1[3]);
    *reinterpret_cast<u32x4*>(h2 + (size_t)row * D_ + c * 8) = o;
  }
}

struct EpiGLU {
  u16* hh; int m0, n0;
  template <int MT, int NTW, int NTHR>
  DI void run(f32x4 (&acc)[MT][NTW], char* lds, int row0, int tile0, int ntw, int fq) const {
    constexpr int RS = 592;
#pragma unroll
    for (int m = 0; m < MT; ++m)
#pragma unroll
      for (int n = 0; n < NTW; ++n) {
        u32x2 o;
        o[0] = pack2(acc[m][n][0], acc[m][n][1]);
        o[1] = pack2(acc[m][n][2], acc[m][n][3]);
        *reinterpret_cast<u32x2*>(lds + (row0 + m * 16) * RS + ((tile0 + n) * 16 + fq * 4) * 2) = o;
      }
    __syncthreads();
    for (int it = threadIdx.x; it < 256 * 18; it += NTHR) {
      const int row = it / 18, c = it % 18;
      const u32x4 g = *reinterpret_cast<const u32x4*>(lds + row * RS + c * 16);
      const u32x4 u = *reinterpret_cast<const u32x4*>(lds + row * RS + 288 + c * 16);
      u32x4 w;
#pragma unroll
      for (int j = 0; j < 4; ++j) w[j] = pack2(bflo(g[j]) * bflo(u[j]), bfhi(g[j]) * bfhi(u[j]));
      *reinterpret_cast<u32x4*>(hh + (size_t)(m0 + row) * F_ + n0 + c * 8) = w;
    }
  }
};

constexpr int LDS_GLU = 256 * 592;
__global__ __launch_bounds__(512) void k_glu(const u16* __restrict__ h2, const u16* __restrict__ wg,
                                              const u16* __restrict__ wu, u16* __restrict__ hh) {
  extern __shared__ __attribute__((aligned(16))) char lds[];
  const int b = blockIdx.x, xcd = b & 7, idx = b >> 3;
  const int bm = idx & 7, bn = xcd * 4 + (idx >> 3);
  const int n0 = bn * 144;
  EpiGLU epi{hh, bm * 256, n0};
  gemm_body<256, 288, 2, 4, 4, 2, 32, 4, 0>(lds, h2, D_, bm * 256, 0, D_ / 32, nullptr, nullptr, nullptr, wg + n0, wu + n0, F_, epi);
}

struct EpiPart {
  float* part; int m0, n0;
  template <int MT, int NTW, int NTHR>
  DI void run(f32x4 (&acc)[MT][NTW], char* lds, int row0, int tile0, int ntw, int fq) const {
    constexpr int RS = 592;
#pragma unroll
    for (int m = 0; m < MT; ++m)
#pragma unroll
      for (int n = 0; n < NTW; ++n)
        if (n < ntw) *reinterpret_cast<f32x4*>(lds + (row0 + m * 16) * RS + ((tile0 + n) * 16 + fq * 4) * 4) = acc[m][n];
    __syncthreads();
    for (int it = threadIdx.x; it < 128 * 36; it += NTHR) {
      const int row = it / 36, c = it % 36;
      f32x4 w = *reinterpret_cast<const f32x4*>(lds + row * RS + c * 16);
      *reinterpret_cast<f32x4*>(part + (size_t)(m0 + row) * D_ + n0 + c * 4) = w;
    }
  }
};

constexpr int LDS_DOWN = 4 * (128 * 64 * 2 + 64 * 144 * 2);
__global__ __launch_bounds__(512) void k_down(const u16* __restrict__ hh, const u16* __restrict__ wd,
                                               float* __restrict__ part) {
  extern __shared__ __attribute__((aligned(16))) char lds[];
  const int b = blockIdx.x, xcd = b & 7, idx = b >> 3;
  const int sk = xcd >> 1, bn = idx & 3, bm = (xcd & 1) * 8 + (idx >> 2);
  const int n0 = bn * 144;
  EpiPart epi{part + (size_t)sk * S_ * D_, bm * 128, n0};
  gemm_body<128, 144, 1, 2, 4, 2, 64, 4, 0>(lds, hh, F_, bm * 128, sk * (F_ / 4), F_ / 4 / 64, nullptr, nullptr, nullptr, wd + n0, wd + n0, D_, epi);
}

__global__ __launch_bounds__(256) void k_final(const float* __restrict__ part, const u16* __restrict__ x1,
                                                const float* __restrict__ mod, u16* __restrict__ out) {
  const int i = (blockIdx.x * 256 + threadIdx.x) * 8;
  if (i >= S_ * D_) return;
  const int col = i % D_;
  const float* gb = mod + 5 * D_ + col;
  u32x4 xv = *reinterpret_cast<const u32x4*>(x1 + i);
  u32x4 o;
#pragma unroll
  for (int j = 0; j < 4; ++j) {
    float a = 0.f, b = 0.f;
#pragma unroll
    for (int s = 0; s < 4; ++s) {
      f32x2 p = *reinterpret_cast<const f32x2*>(part + (size_t)s * S_ * D_ + i + 2 * j);
      a += p[0]; b += p[1];
    }
    o[j] = pack2(bflo(xv[j]) + gb[2 * j] * a, bfhi(xv[j]) + gb[2 * j + 1] * b);
  }
  *reinterpret_cast<u32x4*>(out + i) = o;
}

extern "C" void kernel_launch(void* const* d_in, const int* in_sizes, int n_in,
                              void* d_out, int out_size, void* d_ws, size_t ws_size,
                              hipStream_t stream) {
  const u16* x = (const u16*)d_in[0];
  const u16* wqkv = (const u16*)d_in[1];
  const u16* wo = (const u16*)d_in[2];
  const u16* wg = (const u16*)d_in[4];
  const u16* wu = (const u16*)d_in[5];
  const u16* wd = (const u16*)d_in[6];
  const u16* c = (const u16*)d_in[7];
  const u16* wmod = (const u16*)d_in[8];
  char* ws = (char*)d_ws;
  float* mod = (float*)(ws + WS_MOD);
  float2* st1 = (float2*)(ws + WS_ST1);
  float2* st2 = (float2*)(ws + WS_ST2);
  u16* Q = (u16*)(ws + WS_Q);
  u16* K = (u16*)(ws + WS_K);
  u16* V = (u16*)(ws + WS_V);
  u16* O = (u16*)(ws + WS_O);
  u16* x1 = (u16*)(ws + WS_X1);
  u16* h2 = (u16*)(ws + WS_H2);
  u16* hh = (u16*)(ws + WS_HH);
  float* part = (float*)(ws + WS_PART);

  (void)hipFuncSetAttribute((const void*)k_qkv, hipFuncAttributeMaxDynamicSharedMemorySize, LDS_QKV);
  (void)hipFuncSetAttribute((const void*)k_proj, hipFuncAttributeMaxDynamicSharedMemorySize, LDS_PROJ);
  (void)hipFuncSetAttribute((const void*)k_glu, hipFuncAttributeMaxDynamicSharedMemorySize, LDS_GLU);
  (void)hipFuncSetAttribute((const void*)k_down, hipFuncAttributeMaxDynamicSharedMemorySize, LDS_DOWN);

  k_prep<<<108 + 512, 256, 0, stream>>>(c, wmod, x, mod, st1);
  k_qkv<<<192, 512, LDS_QKV, stream>>>(x, wqkv, st1, mod, Q);
  k_attn<<<32 * 16, 512, 0, stream>>>(Q, K, V, O);
  k_proj<<<256, 192, LDS_PROJ, stream>>>(O, wo, x, mod, x1, st2);
  k_ln2<<<512, 256, 0, stream>>>(x1, st2, mod, h2);
  k_glu<<<256, 512, LDS_GLU, stream>>>(h2, wg, wu, hh);
  k_down<<<256, 512, LDS_DOWN, stream>>>(hh, wd, part);
  k_final<<<(S_ * D_ / 8 + 255) / 256, 256, 0, stream>>>(part, x1, mod, (u16*)d_out);
}
```

```cpp
#include <hip/hip_runtime.h>
#include <hip/hip_bf16.h>
#include <stdint.h>
#include <type_traits>

typedef __attribute__((ext_vector_type(8))) short bf16x8;
typedef __attribute__((ext_vector_type(4))) short s16x4;
typedef __attribute__((ext_vector_type(4))) float f32x4;
typedef __attribute__((ext_vector_type(2))) float f32x2;
typedef __attribute__((ext_vector_type(16))) float f32x16;
typedef __attribute__((ext_vector_type(4))) unsigned u32x4;
typedef __attribute__((ext_vector_type(2))) unsigned u32x2;
typedef __attribute__((ext_vector_type(2))) __bf16 bf16x2_t;
typedef unsigned short u16;

#define DI __device__ __forceinline__

constexpr int S_ = 2048, D_ = 576, H_ = 32, DH_ = 18, F_ = 4608;
constexpr int HP = 32;
constexpr float QSCALE = 0.23570226039551584f * 1.4426950408889634f;

DI float bf2f(u16 v) { return __uint_as_float(((unsigned)v) << 16); }
DI float bflo(unsigned v) { return __uint_as_float(v << 16); }
DI float bfhi(unsigned v) { return __uint_as_float(v & 0xffff0000u); }
DI unsigned pack2(float a, float b) {
  f32x2 v = {a, b};
  bf16x2_t r = __builtin_convertvector(v, bf16x2_t);
  return __builtin_bit_cast(unsigned, r);
}
DI u16 f2bf(float a) { return (u16)(pack2(a, 0.f) & 0xffffu); }
DI float rbf(float a) { return bflo(pack2(a, 0.f) & 0xffffu); }

typedef __attribute__((address_space(3))) s16x4* lds_s16x4_ptr;
DI s16x4 tr_read(const char* p) {
  return __builtin_amdgcn_ds_read_tr16_b64_v4i16((lds_s16x4_ptr)(p));
}

constexpr size_t WS_MOD = 0;
constexpr size_t WS_ST1 = 16384;
constexpr size_t WS_ST2 = WS_ST1 + 2048 * 8;
constexpr size_t WS_Q = 1 << 20;
constexpr size_t WS_K = WS_Q + (size_t)H_ * S_ * HP * 2;
constexpr size_t WS_V = WS_K + (size_t)H_ * S_ * HP * 2;
constexpr size_t WS_O = WS_V + (size_t)H_ * S_ * HP * 2;
constexpr size_t WS_X1 = WS_O + (size_t)S_ * D_ * 2;
constexpr size_t WS_H2 = WS_X1 + (size_t)S_ * D_ * 2;
constexpr size_t WS_HH = WS_H2 + (size_t)S_ * D_ * 2;
constexpr size_t WS_PART = WS_HH + (size_t)S_ * F_ * 2;

__global__ __launch_bounds__(256) void k_prep(const u16* __restrict__ c, const u16* __restrict__ wmod,
                                               const u16* __restrict__ x, float* __restrict__ mod,
                                               float2* __restrict__ st1) {
  __shared__ float red[64][33];
  const int t = threadIdx.x;
  if (blockIdx.x < 108) {
    const int n0 = blockIdx.x * 32 + (t & 3) * 8;
    const int ks = t >> 2;
    float acc[8];
#pragma unroll
    for (int j = 0; j < 8; ++j) acc[j] = 0.f;
#pragma unroll
    for (int r = 0; r < 9; ++r) {
      const int k = ks * 9 + r;
      float cv = bf2f(c[k]);
      float sv = rbf(cv / (1.f + __expf(-cv)));
      u32x4 w = *reinterpret_cast<const u32x4*>(wmod + (size_t)k * 3456 + n0);
#pragma unroll
      for (int j = 0; j < 4; ++j) {
        acc[2 * j] += sv * bflo(w[j]);
        acc[2 * j + 1] += sv * bfhi(w[j]);
      }
    }
#pragma unroll
    for (int j = 0; j < 8; ++j) red[ks][(t & 3) * 8 + j] = acc[j];
    __syncthreads();
    if (t < 32) {
      float s = 0.f;
#pragma unroll 8
      for (int i = 0; i < 64; ++i) s += red[i][t];
      mod[blockIdx.x * 32 + t] = rbf(s);
    }
  } else {
    const int lane = t & 63, w = t >> 6;
    for (int row = (blockIdx.x - 108) * 4 + w; row < S_; row += (gridDim.x - 108) * 4) {
      const u16* xr = x + (size_t)row * D_;
      u32x4 a = *reinterpret_cast<const u32x4*>(xr + lane * 8);
      u32x4 b = {0, 0, 0, 0};
      if (lane < 8) b = *reinterpret_cast<const u32x4*>(xr + 512 + lane * 8);
      float v[16];
#pragma unroll
      for (int j = 0; j < 4; ++j) {
        v[2 * j] = bflo(a[j]); v[2 * j + 1] = bfhi(a[j]);
        v[8 + 2 * j] = bflo(b[j]); v[8 + 2 * j + 1] = bfhi(b[j]);
      }
      float s = 0.f;
#pragma unroll
      for (int j = 0; j < 16; ++j) s += v[j];
#pragma unroll
      for (int o = 32; o >= 1; o >>= 1) s += __shfl_xor(s, o);
      const float mean = s * (1.f / D_);
      float q = 0.f;
#pragma unroll
      for (int j = 0; j < 8; ++j) { float d = v[j] - mean; q += d * d; }
      if (lane < 8) {
#pragma unroll
        for (int j = 8; j < 16; ++j) { float d = v[j] - mean; q += d * d; }
      }
#pragma unroll
      for (int o = 32; o >= 1; o >>= 1) q += __shfl_xor(q, o);
      if (lane == 0) st1[row] = make_float2(mean, rsqrtf(q * (1.f / D_) + 1e-5f));
    }
  }
}

#define WAIT_LGKM0() asm volatile("s_waitcnt lgkmcnt(0)" ::: "memory")
#define STAMP(i)
#define RAW_BAR() __builtin_amdgcn_s_barrier()
template <int N> DI void wait_vm() { asm volatile("s_waitcnt vmcnt(%0)" ::"n"(N) : "memory"); }
template <int OFF> DI void lds_tr(u32x2& d, unsigned a) { asm volatile("ds_read_b64_tr_b16 %0, %1 offset:%2" : "=v"(d) : "v"(a), "n"(OFF)); }
template <int OFF> DI void lds_128(u32x4& d, unsigned a) { asm volatile("ds_read_b128 %0, %1 offset:%2" : "=v"(d) : "v"(a), "n"(OFF)); }
DI void pin4(u32x4& a) { asm volatile("" : "+v"(a)); }
template <int N> DI void wait_l2(u32x2& a, u32x2& b) { asm volatile("s_waitcnt lgkmcnt(%2)" : "+v"(a), "+v"(b) : "n"(N)); }
template <int N> DI void wait_l4(u32x2& a, u32x2& b, u32x4& c, u32x4& d) { asm volatile("s_waitcnt lgkmcnt(%4)" : "+v"(a), "+v"(b), "+v"(c), "+v"(d) : "n"(N)); }

template <int BK> DI int a_swz(int row) { return BK >= 64 ? ((row >> 1) & 7) : ((-(row >> 2)) & 3); }

template <int I, int N, class F> DI void static_for(F&& f) {
  if constexpr (I < N) { f(std::integral_constant<int, I>{}); static_for<I + 1, N>(f); }
}

template <int BM, int BN, int NIMG, int MT, int WM, int WN, int BK, int NST, int LNMODE  , class Epi>
DI void gemm_body(char* lds, const u16* __restrict__ A, int lda, int m0, int kbeg, int nk,
                  const float2* __restrict__ stats, const float* __restrict__ mscale, const float* __restrict__ mshift,
                  const u16* __restrict__ B0, const u16* __restrict__ B1, int ldb, Epi& epi) {
  constexpr int NW = WM * WN, NTHR = 64 * NW;
  constexpr int NT = BN / 16, NTW0 = (NT + WN - 1) / WN, NTWL = NT - (WN - 1) * NTW0, BNI = BN / NIMG, NTI = NT / NIMG;
  constexpr int ABYTES = BM * BK * 2, BBYTES = BK * BN * 2, STAGE = ABYTES + BBYTES;
  constexpr int AINS = ABYTES / 1024, BINS = BBYTES / 1024;
  constexpr int BINS_I = BINS / NIMG;
  constexpr int TINS = AINS + BINS;
  constexpr int G = (TINS + NW - 1) / NW;
  constexpr int SUB = BK / 32;
  constexpr int CPR = BK >= 64 ? 8 : 4;
  constexpr int ARB = CPR * 16;
  static_assert(BBYTES % (1024 * NIMG) == 0 && ABYTES % 1024 == 0 && BM == WM * MT * 16, "tile");
  static_assert(NIMG == 1 || NTW0 == NTI, "img");
  const int t = threadIdx.x, lane = t & 63;
  const int wid = __builtin_amdgcn_readfirstlane(t >> 6);
  const int wr = wid % WM, wn = wid / WM, fr = lane & 15, fq = lane >> 4;
  float* lnp = reinterpret_cast<float*>(lds + NST * STAGE);

  STAMP(0);
  unsigned goff[G];
#pragma unroll
  for (int ii = 0; ii < G; ++ii) {
    int i = wid + ii * NW;
    if (i >= TINS) i -= TINS;
    if (i < AINS) {
      const int j = i * 64 + lane;
      const int si = j / (BM * CPR), row = (j / CPR) % BM, pos = j % CPR;
      goff[ii] = (unsigned)((row * lda + si * 64 + (pos ^ a_swz<BK>(row)) * 8) * 2);
    } else {
      const int j = ((i - AINS) % BINS_I) * 64 + lane;
      const int kb = j / BNI, rem = j % BNI;
      const int sub = rem >> 4, r = (rem >> 1) & 7, hf = rem & 1;
      goff[ii] = (unsigned)(((kb * 8 + (r ^ ((kb & 1) << 2))) * ldb + sub * 16 + hf * 8) * 2);
    }
  }
  const char* Abase = (const char*)(A + (size_t)m0 * lda + kbeg);
  const char* B0base = (const char*)(B0 + (size_t)kbeg * ldb);
  const char* B1base = (const char*)(B1 + (size_t)kbeg * ldb);

  auto issue_one = [&](int kt, int ii) {
    char* buf = lds + (kt % NST) * STAGE;
    int i = wid + ii * NW;
    if (i >= TINS) i -= TINS;
    const char* base = (i < AINS) ? (Abase + kt * BK * 2)
                                  : (((NIMG == 2 && i - AINS >= BINS_I) ? B1base : B0base) + (size_t)kt * BK * ldb * 2);
    __builtin_amdgcn_global_load_lds((const unsigned*)(base + goff[ii]),
                                     (__attribute__((address_space(3))) unsigned*)(buf + (i * 64 + lane) * 16), 16, 0, 0);
  };

#pragma unroll
  for (int st = 0; st < NST - 2; ++st)
    if (st < nk) {
#pragma unroll
      for (int ii = 0; ii < G; ++ii) issue_one(st, ii);
    }

  float ra[MT], rb[MT];
  if constexpr (LNMODE != 0) {
    for (int i = t; i < D_; i += NTHR) { lnp[i] = mscale[i] + 1.f; lnp[D_ + i] = mshift[i]; }
#pragma unroll
    for (int m = 0; m < MT; ++m) {
      float2 s = stats[m0 + (wr * MT + m) * 16 + fr];
      ra[m] = s.y; rb[m] = -s.x * s.y;
    }
    wait_vm<0>();
    WAIT_LGKM0();
  }

  f32x4 acc[MT][NTW0];
#pragma unroll
  for (int m = 0; m < MT; ++m)
#pragma unroll
    for (int n = 0; n < NTW0; ++n) acc[m][n] = f32x4{0.f, 0.f, 0.f, 0.f};

  const unsigned lbase = (unsigned)(size_t)lds;
  const int q4 = fr >> 2, p4 = lane & 3, sw = (fq & 1) << 2;
  const int arow = wr * MT * 16 + fr;
  const unsigned aoffb = arow * ARB;
  const int tile0 = wn * NTW0;
  const unsigned bimg = (unsigned)((tile0 / NTI) * (BBYTES / NIMG) + (tile0 % NTI) * 256);
  const unsigned boff_lo = ABYTES + bimg + fq * (BNI * 16) + ((q4 ^ sw) << 5) + p4 * 8;
  const unsigned boff_hi = ABYTES + bimg + fq * (BNI * 16) + (((q4 + 4) ^ sw) << 5) + p4 * 8;

  auto mainloop = [&](auto NTWC) {
    constexpr int NTW = decltype(NTWC)::value;
    constexpr int T = SUB * NTW;
    constexpr int PF = (T % 3 == 0) ? 3 : 2;
    u32x2 blo[PF], bhi[PF];
    u32x4 af[2][MT];
    auto rda = [&](unsigned sbx, auto S2, auto PAR) {
      constexpr int s2 = decltype(S2)::value, par = decltype(PAR)::value;
      const int ch = (s2 & (CPR / 4 - 1)) * 4 + fq;
      const unsigned base = sbx + (s2 / (CPR / 4)) * (BM * ARB) + aoffb;
      lds_128<0>(af[par][0], base + ((ch ^ a_swz<BK>(arow)) << 4));
      if constexpr (MT > 1) lds_128<16 * ARB>(af[par][MT > 1 ? 1 : 0], base + ((ch ^ a_swz<BK>(arow + 16)) << 4));
      if constexpr (MT > 2) {
        lds_128<32 * ARB>(af[par][MT > 2 ? 2 : 0], base + ((ch ^ a_swz<BK>(arow + 32)) << 4));
        lds_128<48 * ARB>(af[par][MT > 2 ? 3 : 0], base + ((ch ^ a_swz<BK>(arow + 48)) << 4));
      }
    };
    auto rdb = [&](unsigned sbx, auto IDX) {
      constexpr int idx = decltype(IDX)::value;
      constexpr int s2 = idx / NTW, n = idx % NTW;
      constexpr int off = s2 * 4 * (BNI * 16) + n * 256;
      lds_tr<off>(blo[idx % PF], sbx + boff_lo);
      lds_tr<off>(bhi[idx % PF], sbx + boff_hi);
    };
    if (NST - 3 < nk - 1) wait_vm<G * (NST - 3)>(); else wait_vm<0>();
    RAW_BAR();
    rda(lbase, std::integral_constant<int, 0>{}, std::integral_constant<int, 0>{});
    static_for<0, PF - 1>([&](auto I) { rdb(lbase, I); });

    auto stage = [&](int kt, auto PAR0, auto LAST_) {
      constexpr int par0 = decltype(PAR0)::value;
      constexpr bool LAST = decltype(LAST_)::value;
      STAMP(2 + 2 * kt);
      const bool more = kt + NST - 2 < nk;
      const unsigned sb = lbase + (kt % NST) * STAGE;
      const unsigned sbn = lbase + ((kt + 1) % NST) * STAGE;
      static_for<0, T>([&](auto IDX) {
        constexpr int idx = decltype(IDX)::value;
        constexpr int s2 = idx / NTW, n = idx % NTW;
        constexpr int par = (par0 + s2) & 1;
        if constexpr (!LAST && idx == T - PF) {
          if (kt + 1 + (NST - 3) < nk) wait_vm<G * (NST - 3)>(); else wait_vm<0>();
          RAW_BAR();
        }
        constexpr bool rdA = (n == NTW - PF) && (s2 + 1 < SUB || !LAST);
        if constexpr (rdA) {
          if constexpr (s2 + 1 < SUB) rda(sb, std::integral_constant<int, s2 + 1>{}, std::integral_constant<int, par ^ 1>{});
          else rda(sbn, std::integral_constant<int, 0>{}, std::integral_constant<int, par ^ 1>{});
        }
        constexpr bool rdB = (idx + PF - 1 < T) || !LAST;
        if constexpr (rdB) {
          if constexpr (idx + PF - 1 < T) rdb(sb, std::integral_constant<int, idx + PF - 1>{});
          else rdb(sbn, std::integral_constant<int, idx + PF - 1 - T>{});
        }
        constexpr int nb = LAST ? (((idx + PF - 1 < T) ? (idx + PF - 1) : (T - 1)) - idx) : (PF - 1);
        constexpr int nA = (n + PF) / NTW - 1 + 0;
        constexpr bool thisA = (n >= NTW - PF) && (n <= NTW - 2) && (s2 + 1 < SUB || !LAST);
        constexpr int cnt = 2 * nb + (thisA ? MT : 0);
        (void)nA;
        if constexpr (n == 0) {
          asm volatile("s_waitcnt lgkmcnt(%0)" ::"n"(cnt));
          pin4(af[par][0]);
          if constexpr (MT > 1) pin4(af[par][MT > 1 ? 1 : 0]);
          if constexpr (MT > 2) { pin4(af[par][MT - 2]); pin4(af[par][MT - 1]); }
          asm volatile("" : "+v"(blo[idx % PF]), "+v"(bhi[idx % PF]));
          if constexpr (LNMODE != 0) {
            const int kc = kbeg + kt * BK + s2 * 32 + fq * 8;
            f32x4 sc0 = *reinterpret_cast<const f32x4*>(lnp + kc), sc1 = *reinterpret_cast<const f32x4*>(lnp + kc + 4);
            f32x4 sh0 = *reinterpret_cast<const f32x4*>(lnp + D_ + kc), sh1 = *reinterpret_cast<const f32x4*>(lnp + D_ + kc + 4);
#pragma unroll
            for (int m = 0; m < MT; ++m) {
              u32x4 r = af[par][m], o;
              const float a = ra[m], b = rb[m];
              o[0] = pack2((bflo(r[0]) * a + b) * sc0[0] + sh0[0], (bfhi(r[0]) * a + b) * sc0[1] + sh0[1]);
              o[1] = pack2((bflo(r[1]) * a + b) * sc0[2] + sh0[2], (bfhi(r[1]) * a + b) * sc0[3] + sh0[3]);
              o[2] = pack2((bflo(r[2]) * a + b) * sc1[0] + sh1[0], (bfhi(r[2]) * a + b) * sc1[1] + sh1[1]);
              o[3] = pack2((bflo(r[3]) * a + b) * sc1[2] + sh1[2], (bfhi(r[3]) * a + b) * sc1[3] + sh1[3]);
              af[par][m] = o;
            }
          }
        } else {
          wait_l2<cnt>(blo[idx % PF], bhi[idx % PF]);
        }
        u32x4 bw = {blo[idx % PF][0], blo[idx % PF][1], bhi[idx % PF][0], bhi[idx % PF][1]};
        const bf16x8 bf = __builtin_bit_cast(bf16x8, bw);
#pragma unroll
        for (int m = 0; m < MT; ++m)
          acc[m][n] = __builtin_amdgcn_mfma_f32_16x16x32_bf16(bf, __builtin_bit_cast(bf16x8, af[par][m]), acc[m][n], 0, 0, 0);
        if constexpr (!LAST) {
          static_for<0, G>([&](auto II) {
            constexpr int ii = decltype(II)::value;
            if constexpr (idx == (ii * (T - PF)) / G) { if (more) issue_one(kt + NST - 2, ii); }
          });
        }
      });
    };
    if constexpr (SUB % 2 == 0) {
      for (int kt = 0; kt < nk - 1; ++kt) stage(kt, std::integral_constant<int, 0>{}, std::false_type{});
      stage(nk - 1, std::integral_constant<int, 0>{}, std::true_type{});
    } else {
      for (int kt = 0; kt < nk - 2; kt += 2) {
        stage(kt, std::integral_constant<int, 0>{}, std::false_type{});
        stage(kt + 1, std::integral_constant<int, 1>{}, std::false_type{});
      }
      stage(nk - 2, std::integral_constant<int, 0>{}, std::false_type{});
      stage(nk - 1, std::integral_constant<int, 1>{}, std::true_type{});
    }
  };
  if constexpr (NTWL != NTW0) {
    if (wn == WN - 1) mainloop(std::integral_constant<int, NTWL>{});
    else mainloop(std::integral_constant<int, NTW0>{});
  } else {
    mainloop(std::integral_constant<int, NTW0>{});
  }
  STAMP(50);
  __syncthreads();
  STAMP(51);
  const int ntw = (wn == WN - 1) ? NTWL : NTW0;
  epi.template run<MT, NTW0, NTHR>(acc, lds, wr * MT * 16 + fr, tile0, ntw, fq);
  STAMP(52);
}

struct EpiQKV {
  u16* qkv;
  int m0, n0;
  template <int MT, int NTW, int NTHR>
  DI void run(f32x4 (&acc)[MT][NTW], char* lds, int row0, int tile0, int ntw, int fq) const {
    constexpr int RS = 296;
    const int which = n0 / D_;
    const float sc = which == 0 ? QSCALE : 1.f;
#pragma unroll
    for (int m = 0; m < MT; ++m)
#pragma unroll
      for (int n = 0; n < NTW; ++n) {
        if (n < ntw) {
          u32x2 o;
          o[0] = pack2(acc[m][n][0] * sc, acc[m][n][1] * sc);
          o[1] = pack2(acc[m][n][2] * sc, acc[m][n][3] * sc);
          *reinterpret_cast<u32x2*>(lds + (row0 + m * 16) * RS + ((tile0 + n) * 16 + fq * 4) * 2) = o;
        }
      }
    __syncthreads();
    const int head0 = (n0 % D_) / DH_;
    const unsigned padw = (which == 2) ? 0x00003f80u : 0u;
    for (int it = threadIdx.x; it < 8 * 128 * 4; it += NTHR) {
      const int ch = it & 3, row = (it >> 2) & 127, hd = it >> 9;
      const unsigned* src = reinterpret_cast<const unsigned*>(lds + row * RS + hd * 36 + ch * 16);
      u32x4 w = {0u, 0u, 0u, 0u};
      if (ch < 2) w = u32x4{src[0], src[1], src[2], src[3]};
      else if (ch == 2) w = u32x4{src[0], padw, 0u, 0u};
      *reinterpret_cast<u32x4*>(qkv + ((size_t)(which * H_ + head0 + hd) * S_ + m0 + row) * HP + ch * 8) = w;
    }
  }
};

constexpr int LDS_QKV = 4 * (128 * 64 * 2 + 64 * 144 * 2) + 2 * D_ * 4;
__global__ __launch_bounds__(512) void k_qkv(const u16* __restrict__ x, const u16* __restrict__ wqkv,
                                              const float2* __restrict__ st1, const float* __restrict__ mod,
                                              u16* __restrict__ qkv) {
  extern __shared__ __attribute__((aligned(16))) char lds[];
  const int b = blockIdx.x;
  const int xcd = b & 7, idx = b >> 3;
  const int bn = (idx % 3) * 4 + (xcd >> 1), bm = (idx / 3) * 2 + (xcd & 1);
  const int n0 = bn * 144;
  EpiQKV epi{qkv, bm * 128, n0};
  gemm_body<128, 144, 1, 2, 4, 2, 64, 4, 1>(lds, x, D_, bm * 128, 0, D_ / 64, st1, mod + D_, mod, wqkv + n0, wqkv + n0, 3 * D_, epi);
}

__global__ __launch_bounds__(512, 4) void k_attn(const u16* __restrict__ Q, const u16* __restrict__ K,
                                                  const u16* __restrict__ V, u16* __restrict__ O,
                                                  const u16* __restrict__ pw0, const u16* __restrict__ pw1,
                                                  const u16* __restrict__ pw2) {
  __shared__ __attribute__((aligned(16))) char lds_all[2 * 4 * 8192];
  const int t = threadIdx.x, lane = t & 63, wid = t >> 6, r32 = lane & 31, h = lane >> 5;
  const int half = wid >> 2, tl = t & 255;
  char* lds = lds_all + half * (4 * 8192);
  {
    const int gi = blockIdx.x * 512 + t;
    u32x4 a0 = reinterpret_cast<const u32x4*>(pw0)[gi];
    u32x4 a1 = reinterpret_cast<const u32x4*>(pw1)[gi];
    u32x4 a2 = reinterpret_cast<const u32x4*>(pw2)[gi];
    unsigned z = a0[0] ^ a1[0] ^ a2[0];
    if (gi < 331776 - 262144) {
      u32x4 b0 = reinterpret_cast<const u32x4*>(pw0)[262144 + gi];
      u32x4 b1 = reinterpret_cast<const u32x4*>(pw1)[262144 + gi];
      u32x4 b2 = reinterpret_cast<const u32x4*>(pw2)[262144 + gi];
      z ^= b0[0] ^ b1[0] ^ b2[0];
    }
    asm volatile("" ::"v"(z));
  }
  const int head = blockIdx.x >> 4, qb = blockIdx.x & 15;
  const int q0 = qb * 128 + (wid & 3) * 32;
  const u16* Qh = Q + (size_t)head * S_ * HP;
  const u16* Kh = K + ((size_t)head * S_ + half * 1024) * HP;
  const u16* Vh = V + ((size_t)head * S_ + half * 1024) * HP;
  const bf16x8 qf0 = *reinterpret_cast<const bf16x8*>(Qh + (size_t)(q0 + r32) * HP + 8 * h);
  const bf16x8 qf1 = *reinterpret_cast<const bf16x8*>(Qh + (size_t)(q0 + r32) * HP + 16 + 8 * h);

  const int w4 = __builtin_amdgcn_readfirstlane(wid & 3);
  const int skey = w4 * 16 + (lane >> 2), spos = lane & 3;
  const unsigned kgo = (unsigned)((skey * HP + (spos ^ ((skey >> 2) & 3)) * 8) * 2);
  const unsigned vgo = (unsigned)((skey * HP + spos * 8) * 2);
  auto issue = [&](int st) {
    char* buf = lds + (st & 3) * 8192;
    __builtin_amdgcn_global_load_lds((const unsigned*)((const char*)Kh + st * (64 * HP * 2) + kgo),
                                     (__attribute__((address_space(3))) unsigned*)(buf + w4 * 1024 + lane * 16), 16, 0, 0);
    __builtin_amdgcn_global_load_lds((const unsigned*)((const char*)Vh + st * (64 * HP * 2) + vgo),
                                     (__attribute__((address_space(3))) unsigned*)(buf + 4096 + w4 * 1024 + lane * 16), 16, 0, 0);
  };

  f32x16 oacc, negm;
  constexpr float THR = 40.f;

  const int kr0 = r32 * 64 + (((0 + h) ^ ((r32 >> 2) & 3)) << 4);
  const int kr1 = r32 * 64 + (((2 + h) ^ ((r32 >> 2) & 3)) << 4);
  const int g = lane >> 4, q4 = (lane & 15) >> 2, p4 = lane & 3;
  const int vr = 4096 + (4 * (g >> 1) + q4) * 64 + (g & 1) * 32 + p4 * 8;
  constexpr int NS = 1024 / 64;

  auto run = [&](auto SAFE_) {
    constexpr bool SAFE = decltype(SAFE_)::value;
#pragma unroll
    for (int i = 0; i < 16; ++i) { oacc[i] = 0.f; negm[i] = 0.f; }
    issue(0); issue(1); issue(2);
    wait_vm<2>();
    RAW_BAR();
    const unsigned lb = (unsigned)(size_t)lds;
    auto qk = [&](u32x4& ka, u32x4& kb) -> f32x16 {
      f32x16 s = __builtin_amdgcn_mfma_f32_32x32x16_bf16(__builtin_bit_cast(bf16x8, ka), qf0, negm, 0, 0, 0);
      return __builtin_amdgcn_mfma_f32_32x32x16_bf16(__builtin_bit_cast(bf16x8, kb), qf1, s, 0, 0, 0);
    };
    f32x16 sc;
    {
      u32x4 ka, kb;
      lds_128<0>(ka, lb + kr0); lds_128<0>(kb, lb + kr1);
      asm volatile("s_waitcnt lgkmcnt(0)" : "+v"(ka), "+v"(kb));
      sc = qk(ka, kb);
      float mx = fmaxf(sc[0], sc[1]);
#pragma unroll
      for (int i = 2; i < 16; ++i) mx = fmaxf(mx, sc[i]);
      mx = fmaxf(mx, __shfl_xor(mx, 32));
#pragma unroll
      for (int i = 0; i < 16; ++i) { negm[i] = -mx; sc[i] -= mx; }
    }
    for (int st = 0; st < NS; ++st) {
      const unsigned cur = lb + (st & 3) * 8192;
      const unsigned nx1 = lb + ((st + 1) & 3) * 8192;
      if (st + 3 < NS) issue(st + 3);
      static_for<0, 2>([&](auto U) {
        constexpr int u = decltype(U)::value;
        u32x4 ka, kb;
        u32x2 v0, v1, v2, v3;
        if constexpr (u == 0) { lds_128<2048>(ka, cur + kr0); lds_128<2048>(kb, cur + kr1); }
        else { lds_128<0>(ka, nx1 + kr0); lds_128<0>(kb, nx1 + kr1); }
        lds_tr<(32 * u) * 64>(v0, cur + vr); lds_tr<(32 * u + 8) * 64>(v1, cur + vr);
        lds_tr<(32 * u + 16) * 64>(v2, cur + vr); lds_tr<(32 * u + 24) * 64>(v3, cur + vr);
        asm volatile("s_waitcnt lgkmcnt(4)" : "+v"(ka), "+v"(kb));
        f32x16 sn = qk(ka, kb);
        if constexpr (SAFE) {
          float mx = fmaxf(sc[0], sc[1]);
#pragma unroll
          for (int i = 2; i < 16; ++i) mx = fmaxf(mx, sc[i]);
          if (!__all(mx <= THR)) {
            mx = fmaxf(mx, __shfl_xor(mx, 32));
            const float d = fmaxf(mx, 0.f);
            const float alpha = __builtin_amdgcn_exp2f(-d);
#pragma unroll
            for (int i = 0; i < 16; ++i) { oacc[i] *= alpha; negm[i] -= d; sc[i] -= d; sn[i] -= d; }
          }
        }
        u32x4 pf0, pf1;
#pragma unroll
        for (int i = 0; i < 4; ++i) {
          pf0[i] = pack2(__builtin_amdgcn_exp2f(sc[2 * i]), __builtin_amdgcn_exp2f(sc[2 * i + 1]));
          pf1[i] = pack2(__builtin_amdgcn_exp2f(sc[8 + 2 * i]), __builtin_amdgcn_exp2f(sc[8 + 2 * i + 1]));
        }
        asm volatile("s_waitcnt lgkmcnt(0)" : "+v"(v0), "+v"(v1), "+v"(v2), "+v"(v3));
        const u32x4 vf0 = {v0[0], v0[1], v1[0], v1[1]}, vf1 = {v2[0], v2[1], v3[0], v3[1]};
        oacc = __builtin_amdgcn_mfma_f32_32x32x16_bf16(__builtin_bit_cast(bf16x8, vf0), __builtin_bit_cast(bf16x8, pf0), oacc, 0, 0, 0);
        oacc = __builtin_amdgcn_mfma_f32_32x32x16_bf16(__builtin_bit_cast(bf16x8, vf1), __builtin_bit_cast(bf16x8, pf1), oacc, 0, 0, 0);
        sc = sn;
      });
      if (st + 3 < NS) wait_vm<2>(); else wait_vm<0>();
      WAIT_LGKM0();
      RAW_BAR();
    }
  };
  run(std::false_type{});
  {
    const float l = oacc[10];
    const bool bad = (h == 0) && !(l < 1.0e30f);
    if (__syncthreads_or(bad)) run(std::true_type{});
  }
  float* xch = reinterpret_cast<float*>(lds_all);
  if (half == 1) {
#pragma unroll
    for (int i = 0; i < 11; ++i) xch[(i * 4 + (wid & 3)) * 64 + lane] = oacc[i];
    xch[(11 * 4 + (wid & 3)) * 64 + lane] = negm[0];
  }
  __syncthreads();
  if (half == 1) return;
  {
    const float nm1 = xch[(11 * 4 + wid) * 64 + lane], nm0 = negm[0];
    const float nm = fminf(nm0, nm1);
    const float f0 = __builtin_amdgcn_exp2f(nm - nm0), f1 = __builtin_amdgcn_exp2f(nm - nm1);
#pragma unroll
    for (int i = 0; i < 11; ++i) oacc[i] = oacc[i] * f0 + xch[(i * 4 + wid) * 64 + lane] * f1;
  }
  const float l = __shfl(oacc[10], r32);
  const float inv = 1.f / l;
  unsigned* op = reinterpret_cast<unsigned*>(O + (size_t)(q0 + r32) * D_ + head * DH_);
  const unsigned w0 = pack2(oacc[0] * inv, oacc[1] * inv), w1 = pack2(oacc[2] * inv, oacc[3] * inv);
  const unsigned w2 = pack2(oacc[4] * inv, oacc[5] * inv), w3 = pack2(oacc[6] * inv, oacc[7] * inv);
  const unsigned w4o = pack2(oacc[8] * inv, oacc[9] * inv);
  op[2 * h] = w0; op[2 * h + 1] = w1; op[4 + 2 * h] = w2; op[5 + 2 * h] = w3;
  if (h == 0) op[8] = w4o;
}

struct EpiProj {
  const u16* x; const float* ga; u16* x1; float2* st2; int m0, n0;
  template <int MT, int NTW, int NTHR>
  DI void run(f32x4 (&acc)[MT][NTW], char* lds, int row0, int tile0, int ntw, int fq) const {
    const int part = (n0 / 144) * 4 + tile0 / NTW;
#pragma unroll
    for (int m = 0; m < MT; ++m) {
      const int row = m0 + row0 + m * 16;
      float s = 0.f;
#pragma unroll
      for (int n = 0; n < NTW; ++n) {
        if (n < ntw) {
          const int col = n0 + (tile0 + n) * 16 + fq * 4;
          u32x2 xv = *reinterpret_cast<const u32x2*>(x + (size_t)row * D_ + col);
          f32x4 g = *reinterpret_cast<const f32x4*>(ga + col);
          u32x2 o;
          o[0] = pack2(bflo(xv[0]) + g[0] * acc[m][n][0], bfhi(xv[0]) + g[1] * acc[m][n][1]);
          o[1] = pack2(bflo(xv[1]) + g[2] * acc[m][n][2], bfhi(xv[1]) + g[3] * acc[m][n][3]);
          *reinterpret_cast<u32x2*>(x1 + (size_t)row * D_ + col) = o;
          acc[m][n] = f32x4{bflo(o[0]), bfhi(o[0]), bflo(o[1]), bfhi(o[1])};
          s += (acc[m][n][0] + acc[m][n][1]) + (acc[m][n][2] + acc[m][n][3]);
        }
      }
      s += __shfl_xor(s, 16); s += __shfl_xor(s, 32);
      const float lm = s / (float)(16 * ntw);
      float q = 0.f;
#pragma unroll
      for (int n = 0; n < NTW; ++n)
        if (n < ntw) {
#pragma unroll
          for (int j = 0; j < 4; ++j) { float d = acc[m][n][j] - lm; q += d * d; }
        }
      q += __shfl_xor(q, 16); q += __shfl_xor(q, 32);
      if (fq == 0) st2[(size_t)row * 16 + part] = make_float2(s, q);
    }
  }
};

constexpr int LDS_PROJ = 4 * (32 * 64 * 2 + 64 * 144 * 2);
__global__ __launch_bounds__(192) void k_proj(const u16* __restrict__ o, const u16* __restrict__ wo,
                                               const u16* __restrict__ x, const float* __restrict__ mod,
                                               u16* __restrict__ x1, float2* __restrict__ st2) {
  extern __shared__ __attribute__((aligned(16))) char lds[];
  const int b = blockIdx.x;
  const int bn = b & 3, bm = b >> 2;
  const int n0 = bn * 144;
  EpiProj epi{x, mod + 2 * D_, x1, st2, bm * 32, n0};
  gemm_body<32, 144, 1, 2, 1, 3, 64, 4, 0>(lds, o, D_, bm * 32, 0, D_ / 64, nullptr, nullptr, nullptr, wo + n0, wo + n0, D_, epi);
}

__global__ __launch_bounds__(256) void k_ln2(const u16* __restrict__ x1, const float2* __restrict__ st2,
                                              const float* __restrict__ mod, u16* __restrict__ h2) {
  const int row = blockIdx.x * 4 + (threadIdx.x >> 6), lane = threadIdx.x & 63;
  const float2 pp = st2[(size_t)row * 16 + (lane & 15)];
  const float cnt = ((lane & 3) == 3) ? 0.f : 48.f;
  float s = (cnt > 0.f) ? pp.x : 0.f;
#pragma unroll
  for (int o = 8; o >= 1; o >>= 1) s += __shfl_xor(s, o);
  const float mean = s * (1.f / D_);
  float d = (cnt > 0.f) ? (pp.x / 48.f - mean) : 0.f;
  float m2 = (cnt > 0.f) ? (pp.y + cnt * d * d) : 0.f;
#pragma unroll
  for (int o = 8; o >= 1; o >>= 1) m2 += __shfl_xor(m2, o);
  const float a = rsqrtf(m2 * (1.f / D_) + 1e-5f), b = -mean * a;
  const float* sb = mod + 3 * D_;
  const float* cb = mod + 4 * D_;
  for (int c = lane; c < 72; c += 64) {
    u32x4 r = *reinterpret_cast<const u32x4*>(x1 + (size_t)row * D_ + c * 8);
    f32x4 sc0 = *reinterpret_cast<const f32x4*>(cb + c * 8), sc1 = *reinterpret_cast<const f32x4*>(cb + c * 8 + 4);
    f32x4 sh0 = *reinterpret_cast<const f32x4*>(sb + c * 8), sh1 = *reinterpret_cast<const f32x4*>(sb + c * 8 + 4);
    u32x4 o;
    o[0] = pack2((bflo(r[0]) * a + b) * (sc0[0] + 1.f) + sh0[0], (bfhi(r[0]) * a + b) * (sc0[1] + 1.f) + sh0[1]);
    o[1] = pack2((bflo(r[1]) * a + b) * (sc0[2] + 1.f) + sh0[2], (bfhi(r[1]) * a + b) * (sc0[3] + 1.f) + sh0[3]);
    o[2] = pack2((bflo(r[2]) * a + b) * (sc1[0] + 1.f) + sh1[0], (bfhi(r[2]) * a + b) * (sc1[1] + 1.f) + sh1[1]);
    o[3] = pack2((bflo(r[3]) * a + b) * (sc1[2] + 1.f) + sh1[2], (bfhi(r[3]) * a + b) * (sc1[3] + 1.f) + sh1[3]);
    *reinterpret_cast<u32x4*>(h2 + (size_t)row * D_ + c * 8) = o;
  }
}

struct EpiGLU {
  u16* hh; int m0, n0;
  template <int MT, int NTW, int NTHR>
  DI void run(f32x4 (&acc)[MT][NTW], char* lds, int row0, int tile0, int ntw, int fq) const {
    constexpr int RS = 592;
#pragma unroll
    for (int m = 0; m < MT; ++m)
#pragma unroll
      for (int n = 0; n < NTW; ++n) {
        u32x2 o;
        o[0] = pack2(acc[m][n][0], acc[m][n][1]);
        o[1] = pack2(acc[m][n][2], acc[m][n][3]);
        *reinterpret_cast<u32x2*>(lds + (row0 + m * 16) * RS + ((tile0 + n) * 16 + fq * 4) * 2) = o;
      }
    __syncthreads();
    for (int it = threadIdx.x; it < 256 * 18; it += NTHR) {
      const int row = it / 18, c = it % 18;
      const u32x4 g = *reinterpret_cast<const u32x4*>(lds + row * RS + c * 16);
      const u32x4 u = *reinterpret_cast<const u32x4*>(lds + row * RS + 288 + c * 16);
      u32x4 w;
#pragma unroll
      for (int j = 0; j < 4; ++j) w[j] = pack2(bflo(g[j]) * bflo(u[j]), bfhi(g[j]) * bfhi(u[j]));
      *reinterpret_cast<u32x4*>(hh + (size_t)(m0 + row) * F_ + n0 + c * 8) = w;
    }
  }
};

constexpr int LDS_GLU = 256 * 592;
__global__ __launch_bounds__(512) void k_glu(const u16* __restrict__ h2, const u16* __restrict__ wg,
                                              const u16* __restrict__ wu, u16* __restrict__ hh) {
  extern __shared__ __attribute__((aligned(16))) char lds[];
  const int b = blockIdx.x, xcd = b & 7, idx = b >> 3;
  const int bm = idx & 7, bn = xcd * 4 + (idx >> 3);
  const int n0 = bn * 144;
  EpiGLU epi{hh, bm * 256, n0};
  gemm_body<256, 288, 2, 4, 4, 2, 32, 4, 0>(lds, h2, D_, bm * 256, 0, D_ / 32, nullptr, nullptr, nullptr, wg + n0, wu + n0, F_, epi);
}

struct EpiPart {
  float* part; int m0, n0;
  template <int MT, int NTW, int NTHR>
  DI void run(f32x4 (&acc)[MT][NTW], char* lds, int row0, int tile0, int ntw, int fq) const {
    constexpr int RS = 592;
#pragma unroll
    for (int m = 0; m < MT; ++m)
#pragma unroll
      for (int n = 0; n < NTW; ++n)
        if (n < ntw) *reinterpret_cast<f32x4*>(lds + (row0 + m * 16) * RS + ((tile0 + n) * 16 + fq * 4) * 4) = acc[m][n];
    __syncthreads();
    for (int it = threadIdx.x; it < 128 * 36; it += NTHR) {
      const int row = it / 36, c = it % 36;
      f32x4 w = *reinterpret_cast<const f32x4*>(lds + row * RS + c * 16);
      *reinterpret_cast<f32x4*>(part + (size_t)(m0 + row) * D_ + n0 + c * 4) = w;
    }
  }
};

constexpr int LDS_DOWN = 4 * (128 * 64 * 2 + 64 * 144 * 2);
__global__ __launch_bounds__(512) void k_down(const u16* __restrict__ hh, const u16* __restrict__ wd,
                                               float* __restrict__ part) {
  extern __shared__ __attribute__((aligned(16))) char lds[];
  const int b = blockIdx.x, xcd = b & 7, idx = b >> 3;
  const int sk = xcd >> 1, bn = idx & 3, bm = (xcd & 1) * 8 + (idx >> 2);
  const int n0 = bn * 144;
  EpiPart epi{part + (size_t)sk * S_ * D_, bm * 128, n0};
  gemm_body<128, 144, 1, 2, 4, 2, 64, 4, 0>(lds, hh, F_, bm * 128, sk * (F_ / 4), F_ / 4 / 64, nullptr, nullptr, nullptr, wd + n0, wd + n0, D_, epi);
}

__global__ __launch_bounds__(256) void k_final(const float* __restrict__ part, const u16* __restrict__ x1,
                                                const float* __restrict__ mod, u16* __restrict__ out) {
  const int i = (blockIdx.x * 256 + threadIdx.x) * 8;
  if (i >= S_ * D_) return;
  const int col = i % D_;
  const float* gb = mod + 5 * D_ + col;
  u32x4 xv = *reinterpret_cast<const u32x4*>(x1 + i);
  u32x4 o;
#pragma unroll
  for (int j = 0; j < 4; ++j) {
    float a = 0.f, b = 0.f;
#pragma unroll
    for (int s = 0; s < 4; ++s) {
      f32x2 p = *reinterpret_cast<const f32x2*>(part + (size_t)s * S_ * D_ + i + 2 * j);
      a += p[0]; b += p[1];
    }
    o[j] = pack2(bflo(xv[j]) + gb[2 * j] * a, bfhi(xv[j]) + gb[2 * j + 1] * b);
  }
  *reinterpret_cast<u32x4*>(out + i) = o;
}

extern "C" void kernel_launch(void* const* d_in, const int* in_sizes, int n_in,
                              void* d_out, int out_size, void* d_ws, size_t ws_size,
                              hipStream_t stream) {
  const u16* x = (const u16*)d_in[0];
  const u16* wqkv = (const u16*)d_in[1];
  const u16* wo = (const u16*)d_in[2];
  const u16* wg = (const u16*)d_in[4];
  const u16* wu = (const u16*)d_in[5];
  const u16* wd = (const u16*)d_in[6];
  const u16* c = (const u16*)d_in[7];
  const u16* wmod = (const u16*)d_in[8];
  char* ws = (char*)d_ws;
  float* mod = (float*)(ws + WS_MOD);
  float2* st1 = (float2*)(ws + WS_ST1);
  float2* st2 = (float2*)(ws + WS_ST2);
  u16* Q = (u16*)(ws + WS_Q);
  u16* K = (u16*)(ws + WS_K);
  u16* V = (u16*)(ws + WS_V);
  u16* O = (u16*)(ws + WS_O);
  u16* x1 = (u16*)(ws + WS_X1);
  u16* h2 = (u16*)(ws + WS_H2);
  u16* hh = (u16*)(ws + WS_HH);
  float* part = (float*)(ws + WS_PART);

  (void)hipFuncSetAttribute((const void*)k_qkv, hipFuncAttributeMaxDynamicSharedMemorySize, LDS_QKV);
  (void)hipFuncSetAttribute((const void*)k_proj, hipFuncAttributeMaxDynamicSharedMemorySize, LDS_PROJ);
  (void)hipFuncSetAttribute((const void*)k_glu, hipFuncAttributeMaxDynamicSharedMemorySize, LDS_GLU);
  (void)hipFuncSetAttribute((const void*)k_down, hipFuncAttributeMaxDynamicSharedMemorySize, LDS_DOWN);

  k_prep<<<108 + 512, 256, 0, stream>>>(c, wmod, x, mod, st1);
  k_qkv<<<192, 512, LDS_QKV, stream>>>(x, wqkv, st1, mod, Q);
  k_attn<<<32 * 16, 512, 0, stream>>>(Q, K, V, O, wg, wu, wd);
  k_proj<<<256, 192, LDS_PROJ, stream>>>(O, wo, x, mod, x1, st2);
  k_ln2<<<512, 256, 0, stream>>>(x1, st2, mod, h2);
  k_glu<<<256, 512, LDS_GLU, stream>>>(h2, wg, wu, hh);
  k_down<<<256, 512, LDS_DOWN, stream>>>(hh, wd, part);
  k_final<<<(S_ * D_ / 8 + 255) / 256, 256, 0, stream>>>(part, x1, mod, (u16*)d_out);
}
```

```cpp
#include <hip/hip_runtime.h>
#include <hip/hip_bf16.h>
#include <stdint.h>
#include <type_traits>

typedef __attribute__((ext_vector_type(8))) short bf16x8;
typedef __attribute__((ext_vector_type(4))) short s16x4;
typedef __attribute__((ext_vector_type(4))) float f32x4;
typedef __attribute__((ext_vector_type(2))) float f32x2;
typedef __attribute__((ext_vector_type(16))) float f32x16;
typedef __attribute__((ext_vector_type(4))) unsigned u32x4;
typedef __attribute__((ext_vector_type(2))) unsigned u32x2;
typedef __attribute__((ext_vector_type(2))) __bf16 bf16x2_t;
typedef unsigned short u16;

#define DI __device__ __forceinline__

constexpr int S_ = 2048, D_ = 576, H_ = 32, DH_ = 18, F_ = 4608;
constexpr int HP = 32;
constexpr float QSCALE = 0.23570226039551584f * 1.4426950408889634f;

DI float bf2f(u16 v) { return __uint_as_float(((unsigned)v) << 16); }
DI float bflo(unsigned v) { return __uint_as_float(v << 16); }
DI float bfhi(unsigned v) { return __uint_as_float(v & 0xffff0000u); }
DI unsigned pack2(float a, float b) {
  f32x2 v = {a, b};
  bf16x2_t r = __builtin_convertvector(v, bf16x2_t);
  return __builtin_bit_cast(unsigned, r);
}
DI u16 f2bf(float a) { return (u16)(pack2(a, 0.f) & 0xffffu); }
DI float rbf(float a) { return bflo(pack2(a, 0.f) & 0xffffu); }

typedef __attribute__((address_space(3))) s16x4* lds_s16x4_ptr;
DI s16x4 tr_read(const char* p) {
  return __builtin_amdgcn_ds_read_tr16_b64_v4i16((lds_s16x4_ptr)(p));
}

constexpr size_t WS_MOD = 0;
constexpr size_t WS_ST1 = 16384;
constexpr size_t WS_ST2 = WS_ST1 + 2048 * 8;
constexpr size_t WS_Q = 1 << 20;
constexpr size_t WS_K = WS_Q + (size_t)H_ * S_ * HP * 2;
constexpr size_t WS_V = WS_K + (size_t)H_ * S_ * HP * 2;
constexpr size_t WS_O = WS_V + (size_t)H_ * S_ * HP * 2;
constexpr size_t WS_X1 = WS_O + (size_t)S_ * D_ * 2;
constexpr size_t WS_H2 = WS_X1 + (size_t)S_ * D_ * 2;
constexpr size_t WS_HH = WS_H2 + (size_t)S_ * D_ * 2;
constexpr size_t WS_PART = WS_HH + (size_t)S_ * F_ * 2;

__global__ __launch_bounds__(256) void k_prep(const u16* __restrict__ c, const u16* __restrict__ wmod,
                                               const u16* __restrict__ x, float* __restrict__ mod,
                                               float2* __restrict__ st1) {
  __shared__ float red[64][33];
  const int t = threadIdx.x;
  if (blockIdx.x < 108) {
    const int n0 = blockIdx.x * 32 + (t & 3) * 8;
    const int ks = t >> 2;
    float acc[8];
#pragma unroll
    for (int j = 0; j < 8; ++j) acc[j] = 0.f;
#pragma unroll
    for (int r = 0; r < 9; ++r) {
      const int k = ks * 9 + r;
      float cv = bf2f(c[k]);
      float sv = rbf(cv / (1.f + __expf(-cv)));
      u32x4 w = *reinterpret_cast<const u32x4*>(wmod + (size_t)k * 3456 + n0);
#pragma unroll
      for (int j = 0; j < 4; ++j) {
        acc[2 * j] += sv * bflo(w[j]);
        acc[2 * j + 1] += sv * bfhi(w[j]);
      }
    }
#pragma unroll
    for (int j = 0; j < 8; ++j) red[ks][(t & 3) * 8 + j] = acc[j];
    __syncthreads();
    if (t < 32) {
      float s = 0.f;
#pragma unroll 8
      for (int i = 0; i < 64; ++i) s += red[i][t];
      mod[blockIdx.x * 32 + t] = rbf(s);
    }
  } else {
    const int lane = t & 63, w = t >> 6;
    for (int row = (blockIdx.x - 108) * 4 + w; row < S_; row += (gridDim.x - 108) * 4) {
      const u16* xr = x + (size_t)row * D_;
      u32x4 a = *reinterpret_cast<const u32x4*>(xr + lane * 8);
      u32x4 b = {0, 0, 0, 0};
      if (lane < 8) b = *reinterpret_cast<const u32x4*>(xr + 512 + lane * 8);
      float v[16];
#pragma unroll
      for (int j = 0; j < 4; ++j) {
        v[2 * j] = bflo(a[j]); v[2 * j + 1] = bfhi(a[j]);
        v[8 + 2 * j] = bflo(b[j]); v[8 + 2 * j + 1] = bfhi(b[j]);
      }
      float s = 0.f;
#pragma unroll
      for (int j = 0; j < 16; ++j) s += v[j];
#pragma unroll
      for (int o = 32; o >= 1; o >>= 1) s += __shfl_xor(s, o);
      const float mean = s * (1.f / D_);
      float q = 0.f;
#pragma unroll
      for (int j = 0; j < 8; ++j) { float d = v[j] - mean; q += d * d; }
      if (lane < 8) {
#pragma unroll
        for (int j = 8; j < 16; ++j) { float d = v[j] - mean; q += d * d; }
      }
#pragma unroll
      for (int o = 32; o >= 1; o >>= 1) q += __shfl_xor(q, o);
      if (lane == 0) st1[row] = make_float2(mean, rsqrtf(q * (1.f / D_) + 1e-5f));
    }
  }
}

#define WAIT_LGKM0() asm volatile("s_waitcnt lgkmcnt(0)" ::: "memory")
#define STAMP(i)
#define RAW_BAR() __builtin_amdgcn_s_barrier()
template <int N> DI void wait_vm() { asm volatile("s_waitcnt vmcnt(%0)" ::"n"(N) : "memory"); }
template <int OFF> DI void lds_tr(u32x2& d, unsigned a) { asm volatile("ds_read_b64_tr_b16 %0, %1 offset:%2" : "=v"(d) : "v"(a), "n"(OFF)); }
template <int OFF> DI void lds_128(u32x4& d, unsigned a) { asm volatile("ds_read_b128 %0, %1 offset:%2" : "=v"(d) : "v"(a), "n"(OFF)); }
DI void pin4(u32x4& a) { asm volatile("" : "+v"(a)); }
template <int N> DI void wait_l2(u32x2& a, u32x2& b) { asm volatile("s_waitcnt lgkmcnt(%2)" : "+v"(a), "+v"(b) : "n"(N)); }
template <int N> DI void wait_l4(u32x2& a, u32x2& b, u32x4& c, u32x4& d) { asm volatile("s_waitcnt lgkmcnt(%4)" : "+v"(a), "+v"(b), "+v"(c), "+v"(d) : "n"(N)); }

template <int BK> DI int a_swz(int row) { return BK >= 64 ? ((row >> 1) & 7) : ((-(row >> 2)) & 3); }

template <int I, int N, class F> DI void static_for(F&& f) {
  if constexpr (I < N) { f(std::integral_constant<int, I>{}); static_for<I + 1, N>(f); }
}

template <int BM, int BN, int NIMG, int MT, int WM, int WN, int BK, int NST, int LNMODE  , class Epi>
DI void gemm_body(char* lds, const u16* __restrict__ A, int lda, int m0, int kbeg, int nk,
                  const float2* __restrict__ stats, const float* __restrict__ mscale, const float* __restrict__ mshift,
                  const u16* __restrict__ B0, const u16* __restrict__ B1, int ldb, Epi& epi) {
  constexpr int NW = WM * WN, NTHR = 64 * NW;
  constexpr int NT = BN / 16, NTW0 = (NT + WN - 1) / WN, NTWL = NT - (WN - 1) * NTW0, BNI = BN / NIMG, NTI = NT / NIMG;
  constexpr int ABYTES = BM * BK * 2, BBYTES = BK * BN * 2, STAGE = ABYTES + BBYTES;
  constexpr int AINS = ABYTES / 1024, BINS = BBYTES / 1024;
  constexpr int BINS_I = BINS / NIMG;
  constexpr int TINS = AINS + BINS;
  constexpr int G = (TINS + NW - 1) / NW;
  constexpr int SUB = BK / 32;
  constexpr int CPR = BK >= 64 ? 8 : 4;
  constexpr int ARB = CPR * 16;
  static_assert(BBYTES % (1024 * NIMG) == 0 && ABYTES % 1024 == 0 && BM == WM * MT * 16, "tile");
  static_assert(NIMG == 1 || NTW0 == NTI, "img");
  const int t = threadIdx.x, lane = t & 63;
  const int wid = __builtin_amdgcn_readfirstlane(t >> 6);
  const int wr = wid % WM, wn = wid / WM, fr = lane & 15, fq = lane >> 4;
  float* lnp = reinterpret_cast<float*>(lds + NST * STAGE);

  STAMP(0);
  unsigned goff[G];
#pragma unroll
  for (int ii = 0; ii < G; ++ii) {
    int i = wid + ii * NW;
    if (i >= TINS) i -= TINS;
    if (i < AINS) {
      const int j = i * 64 + lane;
      const int si = j / (BM * CPR), row = (j / CPR) % BM, pos = j % CPR;
      goff[ii] = (unsigned)((row * lda + si * 64 + (pos ^ a_swz<BK>(row)) * 8) * 2);
    } else {
      const int j = ((i - AINS) % BINS_I) * 64 + lane;
      const int kb = j / BNI, rem = j % BNI;
      const int sub = rem >> 4, r = (rem >> 1) & 7, hf = rem & 1;
      goff[ii] = (unsigned)(((kb * 8 + (r ^ ((kb & 1) << 2))) * ldb + sub * 16 + hf * 8) * 2);
    }
  }
  const char* Abase = (const char*)(A + (size_t)m0 * lda + kbeg);
  const char* B0base = (const char*)(B0 + (size_t)kbeg * ldb);
  const char* B1base = (const char*)(B1 + (size_t)kbeg * ldb);

  auto issue_one = [&](int kt, int ii) {
    char* buf = lds + (kt % NST) * STAGE;
    int i = wid + ii * NW;
    if (i >= TINS) i -= TINS;
    const char* base = (i < AINS) ? (Abase + kt * BK * 2)
                                  : (((NIMG == 2 && i - AINS >= BINS_I) ? B1base : B0base) + (size_t)kt * BK * ldb * 2);
    __builtin_amdgcn_global_load_lds((const unsigned*)(base + goff[ii]),
                                     (__attribute__((address_space(3))) unsigned*)(buf + (i * 64 + lane) * 16), 16, 0, 0);
  };

#pragma unroll
  for (int st = 0; st < NST - 2; ++st)
    if (st < nk) {
#pragma unroll
      for (int ii = 0; ii < G; ++ii) issue_one(st, ii);
    }

  float ra[MT], rb[MT];
  if constexpr (LNMODE != 0) {
    for (int i = t; i < D_; i += NTHR) { lnp[i] = mscale[i] + 1.f; lnp[D_ + i] = mshift[i]; }
#pragma unroll
    for (int m = 0; m < MT; ++m) {
      float2 s = stats[m0 + (wr * MT + m) * 16 + fr];
      ra[m] = s.y; rb[m] = -s.x * s.y;
    }
    wait_vm<0>();
    WAIT_LGKM0();
  }

  f32x4 acc[MT][NTW0];
#pragma unroll
  for (int m = 0; m < MT; ++m)
#pragma unroll
    for (int n = 0; n < NTW0; ++n) acc[m][n] = f32x4{0.f, 0.f, 0.f, 0.f};

  const unsigned lbase = (unsigned)(size_t)lds;
  const int q4 = fr >> 2, p4 = lane & 3, sw = (fq & 1) << 2;
  const int arow = wr * MT * 16 + fr;
  const unsigned aoffb = arow * ARB;
  const int tile0 = wn * NTW0;
  const unsigned bimg = (unsigned)((tile0 / NTI) * (BBYTES / NIMG) + (tile0 % NTI) * 256);
  const unsigned boff_lo = ABYTES + bimg + fq * (BNI * 16) + ((q4 ^ sw) << 5) + p4 * 8;
  const unsigned boff_hi = ABYTES + bimg + fq * (BNI * 16) + (((q4 + 4) ^ sw) << 5) + p4 * 8;

  auto mainloop = [&](auto NTWC) {
    constexpr int NTW = decltype(NTWC)::value;
    constexpr int T = SUB * NTW;
    constexpr int PF = (T % 3 == 0) ? 3 : 2;
    u32x2 blo[PF], bhi[PF];
    u32x4 af[2][MT];
    auto rda = [&](unsigned sbx, auto S2, auto PAR) {
      constexpr int s2 = decltype(S2)::value, par = decltype(PAR)::value;
      const int ch = (s2 & (CPR / 4 - 1)) * 4 + fq;
      const unsigned base = sbx + (s2 / (CPR / 4)) * (BM * ARB) + aoffb;
      lds_128<0>(af[par][0], base + ((ch ^ a_swz<BK>(arow)) << 4));
      if constexpr (MT > 1) lds_128<16 * ARB>(af[par][MT > 1 ? 1 : 0], base + ((ch ^ a_swz<BK>(arow + 16)) << 4));
      if constexpr (MT > 2) {
        lds_128<32 * ARB>(af[par][MT > 2 ? 2 : 0], base + ((ch ^ a_swz<BK>(arow + 32)) << 4));
        lds_128<48 * ARB>(af[par][MT > 2 ? 3 : 0], base + ((ch ^ a_swz<BK>(arow + 48)) << 4));
      }
    };
    auto rdb = [&](unsigned sbx, auto IDX) {
      constexpr int idx = decltype(IDX)::value;
      constexpr int s2 = idx / NTW, n = idx % NTW;
      constexpr int off = s2 * 4 * (BNI * 16) + n * 256;
      lds_tr<off>(blo[idx % PF], sbx + boff_lo);
      lds_tr<off>(bhi[idx % PF], sbx + boff_hi);
    };
    if (NST - 3 < nk - 1) wait_vm<G * (NST - 3)>(); else wait_vm<0>();
    RAW_BAR();
    rda(lbase, std::integral_constant<int, 0>{}, std::integral_constant<int, 0>{});
    static_for<0, PF - 1>([&](auto I) { rdb(lbase, I); });

    auto stage = [&](int kt, auto PAR0, auto LAST_) {
      constexpr int par0 = decltype(PAR0)::value;
      constexpr bool LAST = decltype(LAST_)::value;
      STAMP(2 + 2 * kt);
      const bool more = kt + NST - 2 < nk;
      const unsigned sb = lbase + (kt % NST) * STAGE;
      const unsigned sbn = lbase + ((kt + 1) % NST) * STAGE;
      static_for<0, T>([&](auto IDX) {
        constexpr int idx = decltype(IDX)::value;
        constexpr int s2 = idx / NTW, n = idx % NTW;
        constexpr int par = (par0 + s2) & 1;
        if constexpr (!LAST && idx == T - PF) {
          if (kt + 1 + (NST - 3) < nk) wait_vm<G * (NST - 3)>(); else wait_vm<0>();
          RAW_BAR();
        }
        constexpr bool rdA = (n == NTW - PF) && (s2 + 1 < SUB || !LAST);
        if constexpr (rdA) {
          if constexpr (s2 + 1 < SUB) rda(sb, std::integral_constant<int, s2 + 1>{}, std::integral_constant<int, par ^ 1>{});
          else rda(sbn, std::integral_constant<int, 0>{}, std::integral_constant<int, par ^ 1>{});
        }
        constexpr bool rdB = (idx + PF - 1 < T) || !LAST;
        if constexpr (rdB) {
          if constexpr (idx + PF - 1 < T) rdb(sb, std::integral_constant<int, idx + PF - 1>{});
          else rdb(sbn, std::integral_constant<int, idx + PF - 1 - T>{});
        }
        constexpr int nb = LAST ? (((idx + PF - 1 < T) ? (idx + PF - 1) : (T - 1)) - idx) : (PF - 1);
        constexpr int nA = (n + PF) / NTW - 1 + 0;
        constexpr bool thisA = (n >= NTW - PF) && (n <= NTW - 2) && (s2 + 1 < SUB || !LAST);
        constexpr int cnt = 2 * nb + (thisA ? MT : 0);
        (void)nA;
        if constexpr (n == 0) {
          asm volatile("s_waitcnt lgkmcnt(%0)" ::"n"(cnt));
          pin4(af[par][0]);
          if constexpr (MT > 1) pin4(af[par][MT > 1 ? 1 : 0]);
          if constexpr (MT > 2) { pin4(af[par][MT - 2]); pin4(af[par][MT - 1]); }
          asm volatile("" : "+v"(blo[idx % PF]), "+v"(bhi[idx % PF]));
          if constexpr (LNMODE != 0) {
            const int kc = kbeg + kt * BK + s2 * 32 + fq * 8;
            f32x4 sc0 = *reinterpret_cast<const f32x4*>(lnp + kc), sc1 = *reinterpret_cast<const f32x4*>(lnp + kc + 4);
            f32x4 sh0 = *reinterpret_cast<const f32x4*>(lnp + D_ + kc), sh1 = *reinterpret_cast<const f32x4*>(lnp + D_ + kc + 4);
#pragma unroll
            for (int m = 0; m < MT; ++m) {
              u32x4 r = af[par][m], o;
              const float a = ra[m], b = rb[m];
              o[0] = pack2((bflo(r[0]) * a + b) * sc0[0] + sh0[0], (bfhi(r[0]) * a + b) * sc0[1] + sh0[1]);
              o[1] = pack2((bflo(r[1]) * a + b) * sc0[2] + sh0[2], (bfhi(r[1]) * a + b) * sc0[3] + sh0[3]);
              o[2] = pack2((bflo(r[2]) * a + b) * sc1[0] + sh1[0], (bfhi(r[2]) * a + b) * sc1[1] + sh1[1]);
              o[3] = pack2((bflo(r[3]) * a + b) * sc1[2] + sh1[2], (bfhi(r[3]) * a + b) * sc1[3] + sh1[3]);
              af[par][m] = o;
            }
          }
        } else {
          wait_l2<cnt>(blo[idx % PF], bhi[idx % PF]);
        }
        u32x4 bw = {blo[idx % PF][0], blo[idx % PF][1], bhi[idx % PF][0], bhi[idx % PF][1]};
        const bf16x8 bf = __builtin_bit_cast(bf16x8, bw);
#pragma unroll
        for (int m = 0; m < MT; ++m)
          acc[m][n] = __builtin_amdgcn_mfma_f32_16x16x32_bf16(bf, __builtin_bit_cast(bf16x8, af[par][m]), acc[m][n], 0, 0, 0);
        if constexpr (!LAST) {
          static_for<0, G>([&](auto II) {
            constexpr int ii = decltype(II)::value;
            if constexpr (idx == (ii * (T - PF)) / G) { if (more) issue_one(kt + NST - 2, ii); }
          });
        }
      });
    };
    if constexpr (SUB % 2 == 0) {
      for (int kt = 0; kt < nk - 1; ++kt) stage(kt, std::integral_constant<int, 0>{}, std::false_type{});
      stage(nk - 1, std::integral_constant<int, 0>{}, std::true_type{});
    } else {
      for (int kt = 0; kt < nk - 2; kt += 2) {
        stage(kt, std::integral_constant<int, 0>{}, std::false_type{});
        stage(kt + 1, std::integral_constant<int, 1>{}, std::false_type{});
      }
      stage(nk - 2, std::integral_constant<int, 0>{}, std::false_type{});
      stage(nk - 1, std::integral_constant<int, 1>{}, std::true_type{});
    }
  };
  if constexpr (NTWL != NTW0) {
    if (wn == WN - 1) mainloop(std::integral_constant<int, NTWL>{});
    else mainloop(std::integral_constant<int, NTW0>{});
  } else {
    mainloop(std::integral_constant<int, NTW0>{});
  }
  STAMP(50);
  __syncthreads();
  STAMP(51);
  const int ntw = (wn == WN - 1) ? NTWL : NTW0;
  epi.template run<MT, NTW0, NTHR>(acc, lds, wr * MT * 16 + fr, tile0, ntw, fq);
  STAMP(52);
}

template <int BM, int NTHR, int MODE>
DI void ln_rows(const u16* __restrict__ src, const float2* __restrict__ stats, const float* __restrict__ csc,
                const float* __restrict__ csh, u16* __restrict__ dst, int m0) {
  const int lane = threadIdx.x & 63, wv = threadIdx.x >> 6;
  constexpr int NWV = NTHR / 64, RPW = BM / NWV;
  f32x4 sc0 = *reinterpret_cast<const f32x4*>(csc + lane * 8), sc1 = *reinterpret_cast<const f32x4*>(csc + lane * 8 + 4);
  f32x4 sh0 = *reinterpret_cast<const f32x4*>(csh + lane * 8), sh1 = *reinterpret_cast<const f32x4*>(csh + lane * 8 + 4);
  const int l8 = lane & 7;
  f32x4 tc0 = *reinterpret_cast<const f32x4*>(csc + 512 + l8 * 8), tc1 = *reinterpret_cast<const f32x4*>(csc + 512 + l8 * 8 + 4);
  f32x4 th0 = *reinterpret_cast<const f32x4*>(csh + 512 + l8 * 8), th1 = *reinterpret_cast<const f32x4*>(csh + 512 + l8 * 8 + 4);
  auto apply = [&](u32x4 r, float a, float b, const f32x4& c0, const f32x4& c1, const f32x4& h0, const f32x4& h1) {
    u32x4 o;
    o[0] = pack2((bflo(r[0]) * a + b) * (c0[0] + 1.f) + h0[0], (bfhi(r[0]) * a + b) * (c0[1] + 1.f) + h0[1]);
    o[1] = pack2((bflo(r[1]) * a + b) * (c0[2] + 1.f) + h0[2], (bfhi(r[1]) * a + b) * (c0[3] + 1.f) + h0[3]);
    o[2] = pack2((bflo(r[2]) * a + b) * (c1[0] + 1.f) + h1[0], (bfhi(r[2]) * a + b) * (c1[1] + 1.f) + h1[1]);
    o[3] = pack2((bflo(r[3]) * a + b) * (c1[2] + 1.f) + h1[2], (bfhi(r[3]) * a + b) * (c1[3] + 1.f) + h1[3]);
    return o;
  };
  for (int r0 = 0; r0 < RPW; r0 += 8) {
    const int rowb = m0 + wv * RPW + r0;
    float a8, b8;
    {
      const int row = rowb + (lane >> 3);
      if constexpr (MODE == 1) { float2 st = stats[row]; a8 = st.y; b8 = -st.x * st.y; }
      else {
        float2 p0 = stats[(size_t)row * 16 + l8 * 2], p1 = stats[(size_t)row * 16 + l8 * 2 + 1];
        const bool v1 = (l8 & 1) == 0;
        float sm = p0.x + (v1 ? p1.x : 0.f);
        sm += __shfl_xor(sm, 1); sm += __shfl_xor(sm, 2); sm += __shfl_xor(sm, 4);
        const float mean = sm * (1.f / D_);
        float d0 = p0.x * (1.f / 48.f) - mean, d1 = p1.x * (1.f / 48.f) - mean;
        float m2 = p0.y + 48.f * d0 * d0 + (v1 ? p1.y + 48.f * d1 * d1 : 0.f);
        m2 += __shfl_xor(m2, 1); m2 += __shfl_xor(m2, 2); m2 += __shfl_xor(m2, 4);
        a8 = rsqrtf(m2 * (1.f / D_) + 1e-5f); b8 = -mean * a8;
      }
    }
    u32x4 rv[8];
#pragma unroll
    for (int i = 0; i < 8; ++i) rv[i] = *reinterpret_cast<const u32x4*>(src + (size_t)(rowb + i) * D_ + lane * 8);
    u32x4 tv = *reinterpret_cast<const u32x4*>(src + (size_t)(rowb + (lane >> 3)) * D_ + 512 + l8 * 8);
#pragma unroll
    for (int i = 0; i < 8; ++i) {
      const float a = __shfl(a8, i * 8), b = __shfl(b8, i * 8);
      *reinterpret_cast<u32x4*>(dst + (size_t)(rowb + i) * D_ + lane * 8) = apply(rv[i], a, b, sc0, sc1, sh0, sh1);
    }
    *reinterpret_cast<u32x4*>(dst + (size_t)(rowb + (lane >> 3)) * D_ + 512 + l8 * 8) = apply(tv, a8, b8, tc0, tc1, th0, th1);
  }
  asm volatile("s_waitcnt vmcnt(0)" ::: "memory");
  __syncthreads();
}

struct EpiQKV {
  u16* qkv;
  int m0, n0;
  template <int MT, int NTW, int NTHR>
  DI void run(f32x4 (&acc)[MT][NTW], char* lds, int row0, int tile0, int ntw, int fq) const {
    constexpr int RS = 296;
    const int which = n0 / D_;
    const float sc = which == 0 ? QSCALE : 1.f;
#pragma unroll
    for (int m = 0; m < MT; ++m)
#pragma unroll
      for (int n = 0; n < NTW; ++n) {
        if (n < ntw) {
          u32x2 o;
          o[0] = pack2(acc[m][n][0] * sc, acc[m][n][1] * sc);
          o[1] = pack2(acc[m][n][2] * sc, acc[m][n][3] * sc);
          *reinterpret_cast<u32x2*>(lds + (row0 + m * 16) * RS + ((tile0 + n) * 16 + fq * 4) * 2) = o;
        }
      }
    __syncthreads();
    const int head0 = (n0 % D_) / DH_;
    const unsigned padw = (which == 2) ? 0x00003f80u : 0u;
    for (int it = threadIdx.x; it < 8 * 128 * 4; it += NTHR) {
      const int ch = it & 3, row = (it >> 2) & 127, hd = it >> 9;
      const unsigned* src = reinterpret_cast<const unsigned*>(lds + row * RS + hd * 36 + ch * 16);
      u32x4 w = {0u, 0u, 0u, 0u};
      if (ch < 2) w = u32x4{src[0], src[1], src[2], src[3]};
      else if (ch == 2) w = u32x4{src[0], padw, 0u, 0u};
      *reinterpret_cast<u32x4*>(qkv + ((size_t)(which * H_ + head0 + hd) * S_ + m0 + row) * HP + ch * 8) = w;
    }
  }
};

constexpr int LDS_QKV = 4 * (128 * 64 * 2 + 64 * 144 * 2) + 2 * D_ * 4;
__global__ __launch_bounds__(512) void k_qkv(const u16* __restrict__ x, const u16* __restrict__ wqkv,
                                              const float2* __restrict__ st1, const float* __restrict__ mod,
                                              u16* h1, u16* __restrict__ qkv) {
  extern __shared__ __attribute__((aligned(16))) char lds[];
  const int b = blockIdx.x;
  const int xcd = b & 7, idx = b >> 3;
  const int bn = (idx % 3) * 4 + (xcd >> 1), bm = (idx / 3) * 2 + (xcd & 1);
  const int n0 = bn * 144;
  EpiQKV epi{qkv, bm * 128, n0};
  ln_rows<128, 512, 1>(x, st1, mod + D_, mod, h1, bm * 128);
  gemm_body<128, 144, 1, 2, 4, 2, 64, 4, 0>(lds, h1, D_, bm * 128, 0, D_ / 64, nullptr, nullptr, nullptr, wqkv + n0, wqkv + n0, 3 * D_, epi);
}

__global__ __launch_bounds__(512, 4) void k_attn(const u16* __restrict__ Q, const u16* __restrict__ K,
                                                  const u16* __restrict__ V, u16* __restrict__ O,
                                                  const u16* __restrict__ pw0, const u16* __restrict__ pw1,
                                                  const u16* __restrict__ pw2) {
  __shared__ __attribute__((aligned(16))) char lds_all[2 * 4 * 8192];
  const int t = threadIdx.x, lane = t & 63, wid = t >> 6, r32 = lane & 31, h = lane >> 5;
  const int half = wid >> 2, tl = t & 255;
  char* lds = lds_all + half * (4 * 8192);
  {
    const int gi = blockIdx.x * 512 + t;
    u32x4 a0 = reinterpret_cast<const u32x4*>(pw0)[gi];
    u32x4 a1 = reinterpret_cast<const u32x4*>(pw1)[gi];
    u32x4 a2 = reinterpret_cast<const u32x4*>(pw2)[gi];
    unsigned z = a0[0] ^ a1[0] ^ a2[0];
    if (gi < 331776 - 262144) {
      u32x4 b0 = reinterpret_cast<const u32x4*>(pw0)[262144 + gi];
      u32x4 b1 = reinterpret_cast<const u32x4*>(pw1)[262144 + gi];
      u32x4 b2 = reinterpret_cast<const u32x4*>(pw2)[262144 + gi];
      z ^= b0[0] ^ b1[0] ^ b2[0];
    }
    asm volatile("" ::"v"(z));
  }
  const int head = blockIdx.x >> 4, qb = blockIdx.x & 15;
  const int q0 = qb * 128 + (wid & 3) * 32;
  const u16* Qh = Q + (size_t)head * S_ * HP;
  const u16* Kh = K + ((size_t)head * S_ + half * 1024) * HP;
  const u16* Vh = V + ((size_t)head * S_ + half * 1024) * HP;
  const bf16x8 qf0 = *reinterpret_cast<const bf16x8*>(Qh + (size_t)(q0 + r32) * HP + 8 * h);
  const bf16x8 qf1 = *reinterpret_cast<const bf16x8*>(Qh + (size_t)(q0 + r32) * HP + 16 + 8 * h);

  const int w4 = __builtin_amdgcn_readfirstlane(wid & 3);
  const int skey = w4 * 16 + (lane >> 2), spos = lane & 3;
  const unsigned kgo = (unsigned)((skey * HP + (spos ^ ((skey >> 2) & 3)) * 8) * 2);
  const unsigned vgo = (unsigned)((skey * HP + spos * 8) * 2);
  auto issue = [&](int st) {
    char* buf = lds + (st & 3) * 8192;
    __builtin_amdgcn_global_load_lds((const unsigned*)((const char*)Kh + st * (64 * HP * 2) + kgo),
                                     (__attribute__((address_space(3))) unsigned*)(buf + w4 * 1024 + lane * 16), 16, 0, 0);
    __builtin_amdgcn_global_load_lds((const unsigned*)((const char*)Vh + st * (64 * HP * 2) + vgo),
                                     (__attribute__((address_space(3))) unsigned*)(buf + 4096 + w4 * 1024 + lane * 16), 16, 0, 0);
  };

  f32x16 oacc, negm;
  constexpr float THR = 40.f;

  const int kr0 = r32 * 64 + (((0 + h) ^ ((r32 >> 2) & 3)) << 4);
  const int kr1 = r32 * 64 + (((2 + h) ^ ((r32 >> 2) & 3)) << 4);
  const int g = lane >> 4, q4 = (lane & 15) >> 2, p4 = lane & 3;
  const int vr = 4096 + (4 * (g >> 1) + q4) * 64 + (g & 1) * 32 + p4 * 8;
  constexpr int NS = 1024 / 64;

  auto run = [&](auto SAFE_) {
    constexpr bool SAFE = decltype(SAFE_)::value;
#pragma unroll
    for (int i = 0; i < 16; ++i) { oacc[i] = 0.f; negm[i] = 0.f; }
    issue(0); issue(1); issue(2);
    wait_vm<2>();
    RAW_BAR();
    const unsigned lb = (unsigned)(size_t)lds;
    auto qk = [&](u32x4& ka, u32x4& kb) -> f32x16 {
      f32x16 s = __builtin_amdgcn_mfma_f32_32x32x16_bf16(__builtin_bit_cast(bf16x8, ka), qf0, negm, 0, 0, 0);
      return __builtin_amdgcn_mfma_f32_32x32x16_bf16(__builtin_bit_cast(bf16x8, kb), qf1, s, 0, 0, 0);
    };
    f32x16 sc;
    {
      u32x4 ka, kb;
      lds_128<0>(ka, lb + kr0); lds_128<0>(kb, lb + kr1);
      asm volatile("s_waitcnt lgkmcnt(0)" : "+v"(ka), "+v"(kb));
      sc = qk(ka, kb);
      float mx = fmaxf(sc[0], sc[1]);
#pragma unroll
      for (int i = 2; i < 16; ++i) mx = fmaxf(mx, sc[i]);
      mx = fmaxf(mx, __shfl_xor(mx, 32));
#pragma unroll
      for (int i = 0; i < 16; ++i) { negm[i] = -mx; sc[i] -= mx; }
    }
    for (int st = 0; st < NS; ++st) {
      const unsigned cur = lb + (st & 3) * 8192;
      const unsigned nx1 = lb + ((st + 1) & 3) * 8192;
      if (st + 3 < NS) issue(st + 3);
      static_for<0, 2>([&](auto U) {
        constexpr int u = decltype(U)::value;
        u32x4 ka, kb;
        u32x2 v0, v1, v2, v3;
        if constexpr (u == 0) { lds_128<2048>(ka, cur + kr0); lds_128<2048>(kb, cur + kr1); }
        else { lds_128<0>(ka, nx1 + kr0); lds_128<0>(kb, nx1 + kr1); }
        lds_tr<(32 * u) * 64>(v0, cur + vr); lds_tr<(32 * u + 8) * 64>(v1, cur + vr);
        lds_tr<(32 * u + 16) * 64>(v2, cur + vr); lds_tr<(32 * u + 24) * 64>(v3, cur + vr);
        asm volatile("s_waitcnt lgkmcnt(4)" : "+v"(ka), "+v"(kb));
        f32x16 sn = qk(ka, kb);
        if constexpr (SAFE) {
          float mx = fmaxf(sc[0], sc[1]);
#pragma unroll
          for (int i = 2; i < 16; ++i) mx = fmaxf(mx, sc[i]);
          if (!__all(mx <= THR)) {
            mx = fmaxf(mx, __shfl_xor(mx, 32));
            const float d = fmaxf(mx, 0.f);
            const float alpha = __builtin_amdgcn_exp2f(-d);
#pragma unroll
            for (int i = 0; i < 16; ++i) { oacc[i] *= alpha; negm[i] -= d; sc[i] -= d; sn[i] -= d; }
          }
        }
        u32x4 pf0, pf1;
#pragma unroll
        for (int i = 0; i < 4; ++i) {
          pf0[i] = pack2(__builtin_amdgcn_exp2f(sc[2 * i]), __builtin_amdgcn_exp2f(sc[2 * i + 1]));
          pf1[i] = pack2(__builtin_amdgcn_exp2f(sc[8 + 2 * i]), __builtin_amdgcn_exp2f(sc[8 + 2 * i + 1]));
        }
        asm volatile("s_waitcnt lgkmcnt(0)" : "+v"(v0), "+v"(v1), "+v"(v2), "+v"(v3));
        const u32x4 vf0 = {v0[0], v0[1], v1[0], v1[1]}, vf1 = {v2[0], v2[1], v3[0], v3[1]};
        oacc = __builtin_amdgcn_mfma_f32_32x32x16_bf16(__builtin_bit_cast(bf16x8, vf0), __builtin_bit_cast(bf16x8, pf0), oacc, 0, 0, 0);
        oacc = __builtin_amdgcn_mfma_f32_32x32x16_bf16(__builtin_bit_cast(bf16x8, vf1), __builtin_bit_cast(bf16x8, pf1), oacc, 0, 0, 0);
        sc = sn;
      });
      if (st + 3 < NS) wait_vm<2>(); else wait_vm<0>();
      WAIT_LGKM0();
      RAW_BAR();
    }
  };
  run(std::false_type{});
  {
    const float l = oacc[10];
    const bool bad = (h == 0) && !(l < 1.0e30f);
    if (__syncthreads_or(bad)) run(std::true_type{});
  }
  float* xch = reinterpret_cast<float*>(lds_all);
  if (half == 1) {
#pragma unroll
    for (int i = 0; i < 11; ++i) xch[(i * 4 + (wid & 3)) * 64 + lane] = oacc[i];
    xch[(11 * 4 + (wid & 3)) * 64 + lane] = negm[0];
  }
  __syncthreads();
  if (half == 1) return;
  {
    const float nm1 = xch[(11 * 4 + wid) * 64 + lane], nm0 = negm[0];
    const float nm = fminf(nm0, nm1);
    const float f0 = __builtin_amdgcn_exp2f(nm - nm0), f1 = __builtin_amdgcn_exp2f(nm - nm1);
#pragma unroll
    for (int i = 0; i < 11; ++i) oacc[i] = oacc[i] * f0 + xch[(i * 4 + wid) * 64 + lane] * f1;
  }
  const float l = __shfl(oacc[10], r32);
  const float inv = 1.f / l;
  unsigned* op = reinterpret_cast<unsigned*>(O + (size_t)(q0 + r32) * D_ + head * DH_);
  const unsigned w0 = pack2(oacc[0] * inv, oacc[1] * inv), w1 = pack2(oacc[2] * inv, oacc[3] * inv);
  const unsigned w2 = pack2(oacc[4] * inv, oacc[5] * inv), w3 = pack2(oacc[6] * inv, oacc[7] * inv);
  const unsigned w4o = pack2(oacc[8] * inv, oacc[9] * inv);
  op[2 * h] = w0; op[2 * h + 1] = w1; op[4 + 2 * h] = w2; op[5 + 2 * h] = w3;
  if (h == 0) op[8] = w4o;
}

struct EpiProj {
  const u16* x; const float* ga; u16* x1; float2* st2; int m0, n0;
  template <int MT, int NTW, int NTHR>
  DI void run(f32x4 (&acc)[MT][NTW], char* lds, int row0, int tile0, int ntw, int fq) const {
    const int part = (n0 / 144) * 4 + tile0 / NTW;
#pragma unroll
    for (int m = 0; m < MT; ++m) {
      const int row = m0 + row0 + m * 16;
      float s = 0.f;
#pragma unroll
      for (int n = 0; n < NTW; ++n) {
        if (n < ntw) {
          const int col = n0 + (tile0 + n) * 16 + fq * 4;
          u32x2 xv = *reinterpret_cast<const u32x2*>(x + (size_t)row * D_ + col);
          f32x4 g = *reinterpret_cast<const f32x4*>(ga + col);
          u32x2 o;
          o[0] = pack2(bflo(xv[0]) + g[0] * acc[m][n][0], bfhi(xv[0]) + g[1] * acc[m][n][1]);
          o[1] = pack2(bflo(xv[1]) + g[2] * acc[m][n][2], bfhi(xv[1]) + g[3] * acc[m][n][3]);
          *reinterpret_cast<u32x2*>(x1 + (size_t)row * D_ + col) = o;
          acc[m][n] = f32x4{bflo(o[0]), bfhi(o[0]), bflo(o[1]), bfhi(o[1])};
          s += (acc[m][n][0] + acc[m][n][1]) + (acc[m][n][2] + acc[m][n][3]);
        }
      }
      s += __shfl_xor(s, 16); s += __shfl_xor(s, 32);
      const float lm = s / (float)(16 * ntw);
      float q = 0.f;
#pragma unroll
      for (int n = 0; n < NTW; ++n)
        if (n < ntw) {
#pragma unroll
          for (int j = 0; j < 4; ++j) { float d = acc[m][n][j] - lm; q += d * d; }
        }
      q += __shfl_xor(q, 16); q += __shfl_xor(q, 32);
      if (fq == 0) st2[(size_t)row * 16 + part] = make_float2(s, q);
    }
  }
};

constexpr int LDS_PROJ = 4 * (32 * 64 * 2 + 64 * 144 * 2);
__global__ __launch_bounds__(192) void k_proj(const u16* __restrict__ o, const u16* __restrict__ wo,
                                               const u16* __restrict__ x, const float* __restrict__ mod,
                                               u16* __restrict__ x1, float2* __restrict__ st2) {
  extern __shared__ __attribute__((aligned(16))) char lds[];
  const int b = blockIdx.x;
  const int bn = b & 3, bm = b >> 2;
  const int n0 = bn * 144;
  EpiProj epi{x, mod + 2 * D_, x1, st2, bm * 32, n0};
  gemm_body<32, 144, 1, 2, 1, 3, 64, 4, 0>(lds, o, D_, bm * 32, 0, D_ / 64, nullptr, nullptr, nullptr, wo + n0, wo + n0, D_, epi);
}

struct EpiGLU {
  u16* hh; int m0, n0;
  template <int MT, int NTW, int NTHR>
  DI void run(f32x4 (&acc)[MT][NTW], char* lds, int row0, int tile0, int ntw, int fq) const {
    constexpr int RS = 592;
#pragma unroll
    for (int m = 0; m < MT; ++m)
#pragma unroll
      for (int n = 0; n < NTW; ++n) {
        u32x2 o;
        o[0] = pack2(acc[m][n][0], acc[m][n][1]);
        o[1] = pack2(acc[m][n][2], acc[m][n][3]);
        *reinterpret_cast<u32x2*>(lds + (row0 + m * 16) * RS + ((tile0 + n) * 16 + fq * 4) * 2) = o;
      }
    __syncthreads();
    for (int it = threadIdx.x; it < 256 * 18; it += NTHR) {
      const int row = it / 18, c = it % 18;
      const u32x4 g = *reinterpret_cast<const u32x4*>(lds + row * RS + c * 16);
      const u32x4 u = *reinterpret_cast<const u32x4*>(lds + row * RS + 288 + c * 16);
      u32x4 w;
#pragma unroll
      for (int j = 0; j < 4; ++j) w[j] = pack2(bflo(g[j]) * bflo(u[j]), bfhi(g[j]) * bfhi(u[j]));
      *reinterpret_cast<u32x4*>(hh + (size_t)(m0 + row) * F_ + n0 + c * 8) = w;
    }
  }
};

constexpr int LDS_GLU = 256 * 592;
__global__ __launch_bounds__(512) void k_glu(const u16* __restrict__ x1, const float2* __restrict__ st2,
                                              const float* __restrict__ mod, u16* h2, const u16* __restrict__ wg,
                                              const u16* __restrict__ wu, u16* __restrict__ hh) {
  extern __shared__ __attribute__((aligned(16))) char lds[];
  const int b = blockIdx.x, xcd = b & 7, idx = b >> 3;
  const int bm = idx & 7, bn = xcd * 4 + (idx >> 3);
  const int n0 = bn * 144;
  EpiGLU epi{hh, bm * 256, n0};
  ln_rows<256, 512, 2>(x1, st2, mod + 4 * D_, mod + 3 * D_, h2, bm * 256);
  gemm_body<256, 288, 2, 4, 4, 2, 32, 4, 0>(lds, h2, D_, bm * 256, 0, D_ / 32, nullptr, nullptr, nullptr, wg + n0, wu + n0, F_, epi);
}

struct EpiPart {
  float* part; int m0, n0;
  template <int MT, int NTW, int NTHR>
  DI void run(f32x4 (&acc)[MT][NTW], char* lds, int row0, int tile0, int ntw, int fq) const {
    constexpr int RS = 592;
#pragma unroll
    for (int m = 0; m < MT; ++m)
#pragma unroll
      for (int n = 0; n < NTW; ++n)
        if (n < ntw) *reinterpret_cast<f32x4*>(lds + (row0 + m * 16) * RS + ((tile0 + n) * 16 + fq * 4) * 4) = acc[m][n];
    __syncthreads();
    for (int it = threadIdx.x; it < 128 * 36; it += NTHR) {
      const int row = it / 36, c = it % 36;
      f32x4 w = *reinterpret_cast<const f32x4*>(lds + row * RS + c * 16);
      *reinterpret_cast<f32x4*>(part + (size_t)(m0 + row) * D_ + n0 + c * 4) = w;
    }
  }
};

constexpr int LDS_DOWN = 4 * (128 * 64 * 2 + 64 * 144 * 2);
__global__ __launch_bounds__(512) void k_down(const u16* __restrict__ hh, const u16* __restrict__ wd,
                                               float* __restrict__ part) {
  extern __shared__ __attribute__((aligned(16))) char lds[];
  const int b = blockIdx.x, xcd = b & 7, idx = b >> 3;
  const int sk = xcd >> 1, bn = idx & 3, bm = (xcd & 1) * 8 + (idx >> 2);
  const int n0 = bn * 144;
  EpiPart epi{part + (size_t)sk * S_ * D_, bm * 128, n0};
  gemm_body<128, 144, 1, 2, 4, 2, 64, 4, 0>(lds, hh, F_, bm * 128, sk * (F_ / 4), F_ / 4 / 64, nullptr, nullptr, nullptr, wd + n0, wd + n0, D_, epi);
}

__global__ __launch_bounds__(256) void k_final(const float* __restrict__ part, const u16* __restrict__ x1,
                                                const float* __restrict__ mod, u16* __restrict__ out) {
  const int i = (blockIdx.x * 256 + threadIdx.x) * 8;
  if (i >= S_ * D_) return;
  const int col = i % D_;
  const float* gb = mod + 5 * D_ + col;
  u32x4 xv = *reinterpret_cast<const u32x4*>(x1 + i);
  u32x4 o;
#pragma unroll
  for (int j = 0; j < 4; ++j) {
    float a = 0.f, b = 0.f;
#pragma unroll
    for (int s = 0; s < 4; ++s) {
      f32x2 p = *reinterpret_cast<const f32x2*>(part + (size_t)s * S_ * D_ + i + 2 * j);
      a += p[0]; b += p[1];
    }
    o[j] = pack2(bflo(xv[j]) + gb[2 * j] * a, bfhi(xv[j]) + gb[2 * j + 1] * b);
  }
  *reinterpret_cast<u32x4*>(out + i) = o;
}

extern "C" void kernel_launch(void* const* d_in, const int* in_sizes, int n_in,
                              void* d_out, int out_size, void* d_ws, size_t ws_size,
                              hipStream_t stream) {
  const u16* x = (const u16*)d_in[0];
  const u16* wqkv = (const u16*)d_in[1];
  const u16* wo = (const u16*)d_in[2];
  const u16* wg = (const u16*)d_in[4];
  const u16* wu = (const u16*)d_in[5];
  const u16* wd = (const u16*)d_in[6];
  const u16* c = (const u16*)d_in[7];
  const u16* wmod = (const u16*)d_in[8];
  char* ws = (char*)d_ws;
  float* mod = (float*)(ws + WS_MOD);
  float2* st1 = (float2*)(ws + WS_ST1);
  float2* st2 = (float2*)(ws + WS_ST2);
  u16* Q = (u16*)(ws + WS_Q);
  u16* K = (u16*)(ws + WS_K);
  u16* V = (u16*)(ws + WS_V);
  u16* O = (u16*)(ws + WS_O);
  u16* x1 = (u16*)(ws + WS_X1);
  u16* h2 = (u16*)(ws + WS_H2);
  u16* hh = (u16*)(ws + WS_HH);
  float* part = (float*)(ws + WS_PART);

  (void)hipFuncSetAttribute((const void*)k_qkv, hipFuncAttributeMaxDynamicSharedMemorySize, LDS_QKV);
  (void)hipFuncSetAttribute((const void*)k_proj, hipFuncAttributeMaxDynamicSharedMemorySize, LDS_PROJ);
  (void)hipFuncSetAttribute((const void*)k_glu, hipFuncAttributeMaxDynamicSharedMemorySize, LDS_GLU);
  (void)hipFuncSetAttribute((const void*)k_down, hipFuncAttributeMaxDynamicSharedMemorySize, LDS_DOWN);

  k_prep<<<108 + 512, 256, 0, stream>>>(c, wmod, x, mod, st1);
  k_qkv<<<192, 512, LDS_QKV, stream>>>(x, wqkv, st1, mod, h2, Q);
  k_attn<<<32 * 16, 512, 0, stream>>>(Q, K, V, O, wg, wu, wd);
  k_proj<<<256, 192, LDS_PROJ, stream>>>(O, wo, x, mod, x1, st2);
  k_glu<<<256, 512, LDS_GLU, stream>>>(x1, st2, mod, h2, wg, wu, hh);
  k_down<<<256, 512, LDS_DOWN, stream>>>(hh, wd, part);
  k_final<<<(S_ * D_ / 8 + 255) / 256, 256, 0, stream>>>(part, x1, mod, (u16*)d_out);
}
```

```cpp
#include <hip/hip_runtime.h>
#include <hip/hip_bf16.h>
#include <stdint.h>
#include <type_traits>


typedef __attribute__((ext_vector_type(8))) short bf16x8;
typedef __attribute__((ext_vector_type(4))) short s16x4;
typedef __attribute__((ext_vector_type(4))) float f32x4;
typedef __attribute__((ext_vector_type(2))) float f32x2;
typedef __attribute__((ext_vector_type(16))) float f32x16;
typedef __attribute__((ext_vector_type(4))) unsigned u32x4;
typedef __attribute__((ext_vector_type(2))) unsigned u32x2;
typedef __attribute__((ext_vector_type(2))) __bf16 bf16x2_t;
typedef unsigned short u16;

#define DI __device__ __forceinline__

constexpr int S_ = 2048, D_ = 576, H_ = 32, DH_ = 18, F_ = 4608;
constexpr int HP = 32;
constexpr float QSCALE = 0.23570226039551584f * 1.4426950408889634f;

DI float bf2f(u16 v) { return __uint_as_float(((unsigned)v) << 16); }
DI float bflo(unsigned v) { return __uint_as_float(v << 16); }
DI float bfhi(unsigned v) { return __uint_as_float(v & 0xffff0000u); }
DI unsigned pack2(float a, float b) {
  f32x2 v = {a, b};
  bf16x2_t r = __builtin_convertvector(v, bf16x2_t);
  return __builtin_bit_cast(unsigned, r);
}
DI u16 f2bf(float a) { return (u16)(pack2(a, 0.f) & 0xffffu); }
DI float rbf(float a) { return bflo(pack2(a, 0.f) & 0xffffu); }

typedef __attribute__((address_space(3))) s16x4* lds_s16x4_ptr;
DI s16x4 tr_read(const char* p) {
  return __builtin_amdgcn_ds_read_tr16_b64_v4i16((lds_s16x4_ptr)(p));
}

constexpr size_t WS_MOD = 0;
constexpr size_t WS_ST1 = 16384;
constexpr size_t WS_ST2 = WS_ST1 + 2048 * 8;
constexpr size_t WS_Q = 1 << 20;
constexpr size_t WS_K = WS_Q + (size_t)H_ * S_ * HP * 2;
constexpr size_t WS_V = WS_K + (size_t)H_ * S_ * HP * 2;
constexpr size_t WS_O = WS_V + (size_t)H_ * S_ * HP * 2;
constexpr size_t WS_X1 = WS_O + (size_t)S_ * D_ * 2;
constexpr size_t WS_H2 = WS_X1 + (size_t)S_ * D_ * 2;
constexpr size_t WS_HH = WS_H2 + (size_t)S_ * D_ * 2;
constexpr size_t WS_PART = WS_HH + (size_t)S_ * F_ * 2;

__global__ __launch_bounds__(256) void k_prep(const u16* __restrict__ c, const u16* __restrict__ wmod,
                                               const u16* __restrict__ x, float* __restrict__ mod,
                                               float2* __restrict__ st1) {
  __shared__ float red[64][33];
  const int t = threadIdx.x;
  if (blockIdx.x < 108) {
    const int n0 = blockIdx.x * 32 + (t & 3) * 8;
    const int ks = t >> 2;
    float acc[8];
#pragma unroll
    for (int j = 0; j < 8; ++j) acc[j] = 0.f;
#pragma unroll
    for (int r = 0; r < 9; ++r) {
      const int k = ks * 9 + r;
      float cv = bf2f(c[k]);
      float sv = rbf(cv / (1.f + __expf(-cv)));
      u32x4 w = *reinterpret_cast<const u32x4*>(wmod + (size_t)k * 3456 + n0);
#pragma unroll
      for (int j = 0; j < 4; ++j) {
        acc[2 * j] += sv * bflo(w[j]);
        acc[2 * j + 1] += sv * bfhi(w[j]);
      }
    }
#pragma unroll
    for (int j = 0; j < 8; ++j) red[ks][(t & 3) * 8 + j] = acc[j];
    __syncthreads();
    if (t < 32) {
      float s = 0.f;
#pragma unroll 8
      for (int i = 0; i < 64; ++i) s += red[i][t];
      mod[blockIdx.x * 32 + t] = rbf(s);
    }
  } else {
    const int lane = t & 63, w = t >> 6;
    for (int row = (blockIdx.x - 108) * 4 + w; row < S_; row += (gridDim.x - 108) * 4) {
      const u16* xr = x + (size_t)row * D_;
      u32x4 a = *reinterpret_cast<const u32x4*>(xr + lane * 8);
      u32x4 b = {0, 0, 0, 0};
      if (lane < 8) b = *reinterpret_cast<const u32x4*>(xr + 512 + lane * 8);
      float v[16];
#pragma unroll
      for (int j = 0; j < 4; ++j) {
        v[2 * j] = bflo(a[j]); v[2 * j + 1] = bfhi(a[j]);
        v[8 + 2 * j] = bflo(b[j]); v[8 + 2 * j + 1] = bfhi(b[j]);
      }
      float s = 0.f;
#pragma unroll
      for (int j = 0; j < 16; ++j) s += v[j];
#pragma unroll
      for (int o = 32; o >= 1; o >>= 1) s += __shfl_xor(s, o);
      const float mean = s * (1.f / D_);
      float q = 0.f;
#pragma unroll
      for (int j = 0; j < 8; ++j) { float d = v[j] - mean; q += d * d; }
      if (lane < 8) {
#pragma unroll
        for (int j = 8; j < 16; ++j) { float d = v[j] - mean; q += d * d; }
      }
#pragma unroll
      for (int o = 32; o >= 1; o >>= 1) q += __shfl_xor(q, o);
      if (lane == 0) st1[row] = make_float2(mean, rsqrtf(q * (1.f / D_) + 1e-5f));
    }
  }
}

#define WAIT_LGKM0() asm volatile("s_waitcnt lgkmcnt(0)" ::: "memory")
#define RAW_BAR() __builtin_amdgcn_s_barrier()
template <int N> DI void wait_vm() { asm volatile("s_waitcnt vmcnt(%0)" ::"n"(N) : "memory"); }
template <int OFF> DI void lds_tr(u32x2& d, unsigned a) { asm volatile("ds_read_b64_tr_b16 %0, %1 offset:%2" : "=v"(d) : "v"(a), "n"(OFF)); }
template <int OFF> DI void lds_128(u32x4& d, unsigned a) { asm volatile("ds_read_b128 %0, %1 offset:%2" : "=v"(d) : "v"(a), "n"(OFF)); }
DI void pin4(u32x4& a) { asm volatile("" : "+v"(a)); }
template <int N> DI void wait_l2(u32x2& a, u32x2& b) { asm volatile("s_waitcnt lgkmcnt(%2)" : "+v"(a), "+v"(b) : "n"(N)); }
template <int N> DI void wait_l4(u32x2& a, u32x2& b, u32x4& c, u32x4& d) { asm volatile("s_waitcnt lgkmcnt(%4)" : "+v"(a), "+v"(b), "+v"(c), "+v"(d) : "n"(N)); }

template <int BK> DI int a_swz(int row) { return BK >= 64 ? ((row >> 1) & 7) : ((-(row >> 2)) & 3); }

template <int I, int N, class F> DI void static_for(F&& f) {
  if constexpr (I < N) { f(std::integral_constant<int, I>{}); static_for<I + 1, N>(f); }
}

template <int BM, int BN, int NIMG, int MT, int WM, int WN, int BK, int NST, int LNMODE  , class Epi>
DI void gemm_body(char* lds, const u16* __restrict__ A, int lda, int m0, int kbeg, int nk,
                  const float2* __restrict__ stats, const float* __restrict__ mscale, const float* __restrict__ mshift,
                  const u16* __restrict__ B0, const u16* __restrict__ B1, int ldb, Epi& epi) {
  constexpr int NW = WM * WN, NTHR = 64 * NW;
  constexpr int NT = BN / 16, NTW0 = (NT + WN - 1) / WN, NTWL = NT - (WN - 1) * NTW0, BNI = BN / NIMG, NTI = NT / NIMG;
  constexpr int ABYTES = BM * BK * 2, BBYTES = BK * BN * 2, STAGE = ABYTES + BBYTES;
  constexpr int AINS = ABYTES / 1024, BINS = BBYTES / 1024;
  constexpr int BINS_I = BINS / NIMG;
  constexpr int TINS = AINS + BINS;
  constexpr int G = (TINS + NW - 1) / NW;
  constexpr int SUB = BK / 32;
  constexpr int CPR = BK >= 64 ? 8 : 4;
  constexpr int ARB = CPR * 16;
  static_assert(BBYTES % (1024 * NIMG) == 0 && ABYTES % 1024 == 0 && BM == WM * MT * 16, "tile");
  static_assert(NIMG == 1 || NTW0 == NTI, "img");
  const int t = threadIdx.x, lane = t & 63;
  const int wid = __builtin_amdgcn_readfirstlane(t >> 6);
  const int wr = wid % WM, wn = wid / WM, fr = lane & 15, fq = lane >> 4;
  float* lnp = reinterpret_cast<float*>(lds + NST * STAGE);
  unsigned goff[G];
#pragma unroll
  for (int ii = 0; ii < G; ++ii) {
    int i = wid + ii * NW;
    if (i >= TINS) i -= TINS;
    if (i < AINS) {
      const int j = i * 64 + lane;
      const int si = j / (BM * CPR), row = (j / CPR) % BM, pos = j % CPR;
      goff[ii] = (unsigned)((row * lda + si * 64 + (pos ^ a_swz<BK>(row)) * 8) * 2);
    } else {
      const int j = ((i - AINS) % BINS_I) * 64 + lane;
      const int kb = j / BNI, rem = j % BNI;
      const int sub = rem >> 4, r = (rem >> 1) & 7, hf = rem & 1;
      goff[ii] = (unsigned)(((kb * 8 + (r ^ ((kb & 1) << 2))) * ldb + sub * 16 + hf * 8) * 2);
    }
  }
  const char* Abase = (const char*)(A + (size_t)m0 * lda + kbeg);
  const char* B0base = (const char*)(B0 + (size_t)kbeg * ldb);
  const char* B1base = (const char*)(B1 + (size_t)kbeg * ldb);

  auto issue_one = [&](int kt, int ii) {
    char* buf = lds + (kt % NST) * STAGE;
    int i = wid + ii * NW;
    if (i >= TINS) i -= TINS;
    const char* base = (i < AINS) ? (Abase + kt * BK * 2)
                                  : (((NIMG == 2 && i - AINS >= BINS_I) ? B1base : B0base) + (size_t)kt * BK * ldb * 2);
    __builtin_amdgcn_global_load_lds((const unsigned*)(base + goff[ii]),
                                     (__attribute__((address_space(3))) unsigned*)(buf + (i * 64 + lane) * 16), 16, 0, 0);
  };

#pragma unroll
  for (int st = 0; st < NST - 2; ++st)
    if (st < nk) {
#pragma unroll
      for (int ii = 0; ii < G; ++ii) issue_one(st, ii);
    }

  float ra[MT], rb[MT];
  if constexpr (LNMODE != 0) {
    for (int i = t; i < D_; i += NTHR) { lnp[i] = mscale[i] + 1.f; lnp[D_ + i] = mshift[i]; }
#pragma unroll
    for (int m = 0; m < MT; ++m) {
      float2 s = stats[m0 + (wr * MT + m) * 16 + fr];
      ra[m] = s.y; rb[m] = -s.x * s.y;
    }
    wait_vm<0>();
    WAIT_LGKM0();
  }

  f32x4 acc[MT][NTW0];
#pragma unroll
  for (int m = 0; m < MT; ++m)
#pragma unroll
    for (int n = 0; n < NTW0; ++n) acc[m][n] = f32x4{0.f, 0.f, 0.f, 0.f};

  const unsigned lbase = (unsigned)(size_t)lds;
  const int q4 = fr >> 2, p4 = lane & 3, sw = (fq & 1) << 2;
  const int arow = wr * MT * 16 + fr;
  const unsigned aoffb = arow * ARB;
  const int tile0 = wn * NTW0;
  const unsigned bimg = (unsigned)((tile0 / NTI) * (BBYTES / NIMG) + (tile0 % NTI) * 256);
  const unsigned boff_lo = ABYTES + bimg + fq * (BNI * 16) + ((q4 ^ sw) << 5) + p4 * 8;
  const unsigned boff_hi = ABYTES + bimg + fq * (BNI * 16) + (((q4 + 4) ^ sw) << 5) + p4 * 8;

  auto mainloop = [&](auto NTWC) {
    constexpr int NTW = decltype(NTWC)::value;
    constexpr int T = SUB * NTW;
    constexpr int PF = (T % 3 == 0) ? 3 : 2;
    u32x2 blo[PF], bhi[PF];
    u32x4 af[2][MT];
    auto rda = [&](unsigned sbx, auto S2, auto PAR) {
      constexpr int s2 = decltype(S2)::value, par = decltype(PAR)::value;
      const int ch = (s2 & (CPR / 4 - 1)) * 4 + fq;
      const unsigned base = sbx + (s2 / (CPR / 4)) * (BM * ARB) + aoffb;
      lds_128<0>(af[par][0], base + ((ch ^ a_swz<BK>(arow)) << 4));
      if constexpr (MT > 1) lds_128<16 * ARB>(af[par][MT > 1 ? 1 : 0], base + ((ch ^ a_swz<BK>(arow + 16)) << 4));
      if constexpr (MT > 2) {
        lds_128<32 * ARB>(af[par][MT > 2 ? 2 : 0], base + ((ch ^ a_swz<BK>(arow + 32)) << 4));
        lds_128<48 * ARB>(af[par][MT > 2 ? 3 : 0], base + ((ch ^ a_swz<BK>(arow + 48)) << 4));
      }
    };
    auto rdb = [&](unsigned sbx, auto IDX) {
      constexpr int idx = decltype(IDX)::value;
      constexpr int s2 = idx / NTW, n = idx % NTW;
      constexpr int off = s2 * 4 * (BNI * 16) + n * 256;
      lds_tr<off>(blo[idx % PF], sbx + boff_lo);
      lds_tr<off>(bhi[idx % PF], sbx + boff_hi);
    };
    if (NST - 3 < nk - 1) wait_vm<G * (NST - 3)>(); else wait_vm<0>();
    RAW_BAR();
    rda(lbase, std::integral_constant<int, 0>{}, std::integral_constant<int, 0>{});
    static_for<0, PF - 1>([&](auto I) { rdb(lbase, I); });

    auto stage = [&](int kt, auto PAR0, auto LAST_) {
      constexpr int par0 = decltype(PAR0)::value;
      constexpr bool LAST = decltype(LAST_)::value;
      const bool more = kt + NST - 2 < nk;
      const unsigned sb = lbase + (kt % NST) * STAGE;
      const unsigned sbn = lbase + ((kt + 1) % NST) * STAGE;
      static_for<0, T>([&](auto IDX) {
        constexpr int idx = decltype(IDX)::value;
        constexpr int s2 = idx / NTW, n = idx % NTW;
        constexpr int par = (par0 + s2) & 1;
        if constexpr (!LAST && idx == T - PF) {
          if (kt + 1 + (NST - 3) < nk) wait_vm<G * (NST - 3)>(); else wait_vm<0>();
          RAW_BAR();
        }
        constexpr bool rdA = (n == NTW - PF) && (s2 + 1 < SUB || !LAST);
        if constexpr (rdA) {
          if constexpr (s2 + 1 < SUB) rda(sb, std::integral_constant<int, s2 + 1>{}, std::integral_constant<int, par ^ 1>{});
          else rda(sbn, std::integral_constant<int, 0>{}, std::integral_constant<int, par ^ 1>{});
        }
        constexpr bool rdB = (idx + PF - 1 < T) || !LAST;
        if constexpr (rdB) {
          if constexpr (idx + PF - 1 < T) rdb(sb, std::integral_constant<int, idx + PF - 1>{});
          else rdb(sbn, std::integral_constant<int, idx + PF - 1 - T>{});
        }
        constexpr int nb = LAST ? (((idx + PF - 1 < T) ? (idx + PF - 1) : (T - 1)) - idx) : (PF - 1);
        constexpr int nA = (n + PF) / NTW - 1 + 0;
        constexpr bool thisA = (n >= NTW - PF) && (n <= NTW - 2) && (s2 + 1 < SUB || !LAST);
        constexpr int cnt = 2 * nb + (thisA ? MT : 0);
        (void)nA;
        if constexpr (n == 0) {
          asm volatile("s_waitcnt lgkmcnt(%0)" ::"n"(cnt));
          pin4(af[par][0]);
          if constexpr (MT > 1) pin4(af[par][MT > 1 ? 1 : 0]);
          if constexpr (MT > 2) { pin4(af[par][MT - 2]); pin4(af[par][MT - 1]); }
          asm volatile("" : "+v"(blo[idx % PF]), "+v"(bhi[idx % PF]));
          if constexpr (LNMODE != 0) {
            const int kc = kbeg + kt * BK + s2 * 32 + fq * 8;
            f32x4 sc0 = *reinterpret_cast<const f32x4*>(lnp + kc), sc1 = *reinterpret_cast<const f32x4*>(lnp + kc + 4);
            f32x4 sh0 = *reinterpret_cast<const f32x4*>(lnp + D_ + kc), sh1 = *reinterpret_cast<const f32x4*>(lnp + D_ + kc + 4);
#pragma unroll
            for (int m = 0; m < MT; ++m) {
              u32x4 r = af[par][m], o;
              const float a = ra[m], b = rb[m];
              o[0] = pack2((bflo(r[0]) * a + b) * sc0[0] + sh0[0], (bfhi(r[0]) * a + b) * sc0[1] + sh0[1]);
              o[1] = pack2((bflo(r[1]) * a + b) * sc0[2] + sh0[2], (bfhi(r[1]) * a + b) * sc0[3] + sh0[3]);
              o[2] = pack2((bflo(r[2]) * a + b) * sc1[0] + sh1[0], (bfhi(r[2]) * a + b) * sc1[1] + sh1[1]);
              o[3] = pack2((bflo(r[3]) * a + b) * sc1[2] + sh1[2], (bfhi(r[3]) * a + b) * sc1[3] + sh1[3]);
              af[par][m] = o;
            }
          }
        } else {
          wait_l2<cnt>(blo[idx % PF], bhi[idx % PF]);
        }
        u32x4 bw = {blo[idx % PF][0], blo[idx % PF][1], bhi[idx % PF][0], bhi[idx % PF][1]};
        const bf16x8 bf = __builtin_bit_cast(bf16x8, bw);
#pragma unroll
        for (int m = 0; m < MT; ++m)
          acc[m][n] = __builtin_amdgcn_mfma_f32_16x16x32_bf16(bf, __builtin_bit_cast(bf16x8, af[par][m]), acc[m][n], 0, 0, 0);
        if constexpr (!LAST) {
          static_for<0, G>([&](auto II) {
            constexpr int ii = decltype(II)::value;
            if constexpr (idx == (ii * (T - PF)) / G) { if (more) issue_one(kt + NST - 2, ii); }
          });
        }
      });
    };
    if constexpr (SUB % 2 == 0) {
      for (int kt = 0; kt < nk - 1; ++kt) stage(kt, std::integral_constant<int, 0>{}, std::false_type{});
      stage(nk - 1, std::integral_constant<int, 0>{}, std::true_type{});
    } else {
      for (int kt = 0; kt < nk - 2; kt += 2) {
        stage(kt, std::integral_constant<int, 0>{}, std::false_type{});
        stage(kt + 1, std::integral_constant<int, 1>{}, std::false_type{});
      }
      stage(nk - 2, std::integral_constant<int, 0>{}, std::false_type{});
      stage(nk - 1, std::integral_constant<int, 1>{}, std::true_type{});
    }
  };
  if constexpr (NTWL != NTW0) {
    if (wn == WN - 1) mainloop(std::integral_constant<int, NTWL>{});
    else mainloop(std::integral_constant<int, NTW0>{});
  } else {
    mainloop(std::integral_constant<int, NTW0>{});
  }
  __syncthreads();
  const int ntw = (wn == WN - 1) ? NTWL : NTW0;
  epi.template run<MT, NTW0, NTHR>(acc, lds, wr * MT * 16 + fr, tile0, ntw, fq);
}

struct EpiQKV {
  u16* qkv;
  int m0, n0;
  template <int MT, int NTW, int NTHR>
  DI void run(f32x4 (&acc)[MT][NTW], char* lds, int row0, int tile0, int ntw, int fq) const {
    constexpr int RS = 296;
    const int which = n0 / D_;
    const float sc = which == 0 ? QSCALE : 1.f;
#pragma unroll
    for (int m = 0; m < MT; ++m)
#pragma unroll
      for (int n = 0; n < NTW; ++n) {
        if (n < ntw) {
          u32x2 o;
          o[0] = pack2(acc[m][n][0] * sc, acc[m][n][1] * sc);
          o[1] = pack2(acc[m][n][2] * sc, acc[m][n][3] * sc);
          *reinterpret_cast<u32x2*>(lds + (row0 + m * 16) * RS + ((tile0 + n) * 16 + fq * 4) * 2) = o;
        }
      }
    __syncthreads();
    const int head0 = (n0 % D_) / DH_;
    const unsigned padw = (which == 2) ? 0x00003f80u : 0u;
    for (int it = threadIdx.x; it < 8 * 128 * 4; it += NTHR) {
      const int ch = it & 3, row = (it >> 2) & 127, hd = it >> 9;
      const unsigned* src = reinterpret_cast<const unsigned*>(lds + row * RS + hd * 36 + ch * 16);
      u32x4 w = {0u, 0u, 0u, 0u};
      if (ch < 2) w = u32x4{src[0], src[1], src[2], src[3]};
      else if (ch == 2) w = u32x4{src[0], padw, 0u, 0u};
      *reinterpret_cast<u32x4*>(qkv + ((size_t)(which * H_ + head0 + hd) * S_ + m0 + row) * HP + ch * 8) = w;
    }
  }
};

constexpr int LDS_QKV = 4 * (128 * 64 * 2 + 64 * 144 * 2) + 2 * D_ * 4;
__global__ __launch_bounds__(512) void k_qkv(const u16* __restrict__ x, const u16* __restrict__ wqkv,
                                              const float2* __restrict__ st1, const float* __restrict__ mod,
                                              u16* __restrict__ qkv) {
  extern __shared__ __attribute__((aligned(16))) char lds[];
  const int b = blockIdx.x;
  const int xcd = b & 7, idx = b >> 3;
  const int bn = (idx % 3) * 4 + (xcd >> 1), bm = (idx / 3) * 2 + (xcd & 1);
  const int n0 = bn * 144;
  EpiQKV epi{qkv, bm * 128, n0};
  gemm_body<128, 144, 1, 2, 4, 2, 64, 4, 1>(lds, x, D_, bm * 128, 0, D_ / 64, st1, mod + D_, mod, wqkv + n0, wqkv + n0, 3 * D_, epi);
}

__global__ __launch_bounds__(512, 4) void k_attn(const u16* __restrict__ Q, const u16* __restrict__ K,
                                                  const u16* __restrict__ V, u16* __restrict__ O,
                                                  const u16* __restrict__ pw0, const u16* __restrict__ pw1,
                                                  const u16* __restrict__ pw2) {
  __shared__ __attribute__((aligned(16))) char lds_all[2 * 4 * 8192];
  const int t = threadIdx.x, lane = t & 63, wid = t >> 6, r32 = lane & 31, h = lane >> 5;
  const int half = wid >> 2, tl = t & 255;
  char* lds = lds_all + half * (4 * 8192);
  {
    const int gi = blockIdx.x * 512 + t;
    u32x4 a0 = reinterpret_cast<const u32x4*>(pw0)[gi];
    u32x4 a1 = reinterpret_cast<const u32x4*>(pw1)[gi];
    u32x4 a2 = reinterpret_cast<const u32x4*>(pw2)[gi];
    unsigned z = a0[0] ^ a1[0] ^ a2[0];
    if (gi < 331776 - 262144) {
      u32x4 b0 = reinterpret_cast<const u32x4*>(pw0)[262144 + gi];
      u32x4 b1 = reinterpret_cast<const u32x4*>(pw1)[262144 + gi];
      u32x4 b2 = reinterpret_cast<const u32x4*>(pw2)[262144 + gi];
      z ^= b0[0] ^ b1[0] ^ b2[0];
    }
    asm volatile("" ::"v"(z));
  }
  const int head = blockIdx.x >> 4, qb = blockIdx.x & 15;
  const int q0 = qb * 128 + (wid & 3) * 32;
  const u16* Qh = Q + (size_t)head * S_ * HP;
  const u16* Kh = K + ((size_t)head * S_ + half * 1024) * HP;
  const u16* Vh = V + ((size_t)head * S_ + half * 1024) * HP;
  const bf16x8 qf0 = *reinterpret_cast<const bf16x8*>(Qh + (size_t)(q0 + r32) * HP + 8 * h);
  const bf16x8 qf1 = *reinterpret_cast<const bf16x8*>(Qh + (size_t)(q0 + r32) * HP + 16 + 8 * h);

  const int w4 = __builtin_amdgcn_readfirstlane(wid & 3);
  const int skey = w4 * 16 + (lane >> 2), spos = lane & 3;
  const unsigned kgo = (unsigned)((skey * HP + (spos ^ ((skey >> 2) & 3)) * 8) * 2);
  const unsigned vgo = (unsigned)((skey * HP + spos * 8) * 2);
  auto issue = [&](int st) {
    char* buf = lds + (st & 3) * 8192;
    __builtin_amdgcn_global_load_lds((const unsigned*)((const char*)Kh + st * (64 * HP * 2) + kgo),
                                     (__attribute__((address_space(3))) unsigned*)(buf + w4 * 1024 + lane * 16), 16, 0, 0);
    __builtin_amdgcn_global_load_lds((const unsigned*)((const char*)Vh + st * (64 * HP * 2) + vgo),
                                     (__attribute__((address_space(3))) unsigned*)(buf + 4096 + w4 * 1024 + lane * 16), 16, 0, 0);
  };

  f32x16 oacc, negm;
  constexpr float THR = 40.f;

  const int kr0 = r32 * 64 + (((0 + h) ^ ((r32 >> 2) & 3)) << 4);
  const int kr1 = r32 * 64 + (((2 + h) ^ ((r32 >> 2) & 3)) << 4);
  const int g = lane >> 4, q4 = (lane & 15) >> 2, p4 = lane & 3;
  const int vr = 4096 + (4 * (g >> 1) + q4) * 64 + (g & 1) * 32 + p4 * 8;
  constexpr int NS = 1024 / 64;

  auto run = [&](auto SAFE_) {
    constexpr bool SAFE = decltype(SAFE_)::value;
#pragma unroll
    for (int i = 0; i < 16; ++i) { oacc[i] = 0.f; negm[i] = 0.f; }
    issue(0); issue(1); issue(2);
    wait_vm<2>();
    RAW_BAR();
    const unsigned lb = (unsigned)(size_t)lds;
    auto qk = [&](u32x4& ka, u32x4& kb) -> f32x16 {
      f32x16 s = __builtin_amdgcn_mfma_f32_32x32x16_bf16(__builtin_bit_cast(bf16x8, ka), qf0, negm, 0, 0, 0);
      return __builtin_amdgcn_mfma_f32_32x32x16_bf16(__builtin_bit_cast(bf16x8, kb), qf1, s, 0, 0, 0);
    };
    f32x16 sc;
    {
      u32x4 ka, kb;
      lds_128<0>(ka, lb + kr0); lds_128<0>(kb, lb + kr1);
      asm volatile("s_waitcnt lgkmcnt(0)" : "+v"(ka), "+v"(kb));
      sc = qk(ka, kb);
      float mx = fmaxf(sc[0], sc[1]);
#pragma unroll
      for (int i = 2; i < 16; ++i) mx = fmaxf(mx, sc[i]);
      mx = fmaxf(mx, __shfl_xor(mx, 32));
#pragma unroll
      for (int i = 0; i < 16; ++i) { negm[i] = -mx; sc[i] -= mx; }
    }
    for (int st = 0; st < NS; ++st) {
      const unsigned cur = lb + (st & 3) * 8192;
      const unsigned nx1 = lb + ((st + 1) & 3) * 8192;
      if (st + 3 < NS) issue(st + 3);
      static_for<0, 2>([&](auto U) {
        constexpr int u = decltype(U)::value;
        u32x4 ka, kb;
        u32x2 v0, v1, v2, v3;
        if constexpr (u == 0) { lds_128<2048>(ka, cur + kr0); lds_128<2048>(kb, cur + kr1); }
        else { lds_128<0>(ka, nx1 + kr0); lds_128<0>(kb, nx1 + kr1); }
        lds_tr<(32 * u) * 64>(v0, cur + vr); lds_tr<(32 * u + 8) * 64>(v1, cur + vr);
        lds_tr<(32 * u + 16) * 64>(v2, cur + vr); lds_tr<(32 * u + 24) * 64>(v3, cur + vr);
        asm volatile("s_waitcnt lgkmcnt(4)" : "+v"(ka), "+v"(kb));
        f32x16 sn = qk(ka, kb);
        if constexpr (SAFE) {
          float mx = fmaxf(sc[0], sc[1]);
#pragma unroll
          for (int i = 2; i < 16; ++i) mx = fmaxf(mx, sc[i]);
          if (!__all(mx <= THR)) {
            mx = fmaxf(mx, __shfl_xor(mx, 32));
            const float d = fmaxf(mx, 0.f);
            const float alpha = __builtin_amdgcn_exp2f(-d);
#pragma unroll
            for (int i = 0; i < 16; ++i) { oacc[i] *= alpha; negm[i] -= d; sc[i] -= d; sn[i] -= d; }
          }
        }
        u32x4 pf0, pf1;
#pragma unroll
        for (int i = 0; i < 4; ++i) {
          pf0[i] = pack2(__builtin_amdgcn_exp2f(sc[2 * i]), __builtin_amdgcn_exp2f(sc[2 * i + 1]));
          pf1[i] = pack2(__builtin_amdgcn_exp2f(sc[8 + 2 * i]), __builtin_amdgcn_exp2f(sc[8 + 2 * i + 1]));
        }
        asm volatile("s_waitcnt lgkmcnt(0)" : "+v"(v0), "+v"(v1), "+v"(v2), "+v"(v3));
        const u32x4 vf0 = {v0[0], v0[1], v1[0], v1[1]}, vf1 = {v2[0], v2[1], v3[0], v3[1]};
        oacc = __builtin_amdgcn_mfma_f32_32x32x16_bf16(__builtin_bit_cast(bf16x8, vf0), __builtin_bit_cast(bf16x8, pf0), oacc, 0, 0, 0);
        oacc = __builtin_amdgcn_mfma_f32_32x32x16_bf16(__builtin_bit_cast(bf16x8, vf1), __builtin_bit_cast(bf16x8, pf1), oacc, 0, 0, 0);
        sc = sn;
      });
      if (st + 3 < NS) wait_vm<2>(); else wait_vm<0>();
      WAIT_LGKM0();
      RAW_BAR();
    }
  };
  run(std::false_type{});
  {
    const float l = oacc[10];
    const bool bad = (h == 0) && !(l < 1.0e30f);
    if (__syncthreads_or(bad)) run(std::true_type{});
  }
  float* xch = reinterpret_cast<float*>(lds_all);
  if (half == 1) {
#pragma unroll
    for (int i = 0; i < 11; ++i) xch[(i * 4 + (wid & 3)) * 64 + lane] = oacc[i];
    xch[(11 * 4 + (wid & 3)) * 64 + lane] = negm[0];
  }
  __syncthreads();
  if (half == 1) return;
  {
    const float nm1 = xch[(11 * 4 + wid) * 64 + lane], nm0 = negm[0];
    const float nm = fminf(nm0, nm1);
    const float f0 = __builtin_amdgcn_exp2f(nm - nm0), f1 = __builtin_amdgcn_exp2f(nm - nm1);
#pragma unroll
    for (int i = 0; i < 11; ++i) oacc[i] = oacc[i] * f0 + xch[(i * 4 + wid) * 64 + lane] * f1;
  }
  const float l = __shfl(oacc[10], r32);
  const float inv = 1.f / l;
  unsigned* op = reinterpret_cast<unsigned*>(O + (size_t)(q0 + r32) * D_ + head * DH_);
  const unsigned w0 = pack2(oacc[0] * inv, oacc[1] * inv), w1 = pack2(oacc[2] * inv, oacc[3] * inv);
  const unsigned w2 = pack2(oacc[4] * inv, oacc[5] * inv), w3 = pack2(oacc[6] * inv, oacc[7] * inv);
  const unsigned w4o = pack2(oacc[8] * inv, oacc[9] * inv);
  op[2 * h] = w0; op[2 * h + 1] = w1; op[4 + 2 * h] = w2; op[5 + 2 * h] = w3;
  if (h == 0) op[8] = w4o;
}

struct EpiProj {
  const u16* x; const float* ga; u16* x1; float2* st2; int m0, n0;
  template <int MT, int NTW, int NTHR>
  DI void run(f32x4 (&acc)[MT][NTW], char* lds, int row0, int tile0, int ntw, int fq) const {
    const int part = (n0 / 144) * 4 + tile0 / NTW;
#pragma unroll
    for (int m = 0; m < MT; ++m) {
      const int row = m0 + row0 + m * 16;
      float s = 0.f;
#pragma unroll
      for (int n = 0; n < NTW; ++n) {
        if (n < ntw) {
          const int col = n0 + (tile0 + n) * 16 + fq * 4;
          u32x2 xv = *reinterpret_cast<const u32x2*>(x + (size_t)row * D_ + col);
          f32x4 g = *reinterpret_cast<const f32x4*>(ga + col);
          u32x2 o;
          o[0] = pack2(bflo(xv[0]) + g[0] * acc[m][n][0], bfhi(xv[0]) + g[1] * acc[m][n][1]);
          o[1] = pack2(bflo(xv[1]) + g[2] * acc[m][n][2], bfhi(xv[1]) + g[3] * acc[m][n][3]);
          *reinterpret_cast<u32x2*>(x1 + (size_t)row * D_ + col) = o;
          acc[m][n] = f32x4{bflo(o[0]), bfhi(o[0]), bflo(o[1]), bfhi(o[1])};
          s += (acc[m][n][0] + acc[m][n][1]) + (acc[m][n][2] + acc[m][n][3]);
        }
      }
      s += __shfl_xor(s, 16); s += __shfl_xor(s, 32);
      const float lm = s / (float)(16 * ntw);
      float q = 0.f;
#pragma unroll
      for (int n = 0; n < NTW; ++n)
        if (n < ntw) {
#pragma unroll
          for (int j = 0; j < 4; ++j) { float d = acc[m][n][j] - lm; q += d * d; }
        }
      q += __shfl_xor(q, 16); q += __shfl_xor(q, 32);
      if (fq == 0) st2[(size_t)row * 16 + part] = make_float2(s, q);
    }
  }
};

constexpr int LDS_PROJ = 4 * (32 * 64 * 2 + 64 * 144 * 2);
__global__ __launch_bounds__(384) void k_proj(const u16* __restrict__ o, const u16* __restrict__ wo,
                                               const u16* __restrict__ x, const float* __restrict__ mod,
                                               u16* __restrict__ x1, float2* __restrict__ st2) {
  extern __shared__ __attribute__((aligned(16))) char lds[];
  const int b = blockIdx.x;
  const int bn = b & 3, bm = b >> 2;
  const int n0 = bn * 144;
  EpiProj epi{x, mod + 2 * D_, x1, st2, bm * 32, n0};
  gemm_body<32, 144, 1, 1, 2, 3, 64, 4, 0>(lds, o, D_, bm * 32, 0, D_ / 64, nullptr, nullptr, nullptr, wo + n0, wo + n0, D_, epi);
}

__global__ __launch_bounds__(256) void k_ln2(const u16* __restrict__ x1, const float2* __restrict__ st2,
                                              const float* __restrict__ mod, u16* __restrict__ h2) {
  const int row = blockIdx.x * 4 + (threadIdx.x >> 6), lane = threadIdx.x & 63;
  const float2 pp = st2[(size_t)row * 16 + (lane & 15)];
  const float cnt = ((lane & 3) == 3) ? 0.f : 48.f;
  float s = (cnt > 0.f) ? pp.x : 0.f;
#pragma unroll
  for (int o = 8; o >= 1; o >>= 1) s += __shfl_xor(s, o);
  const float mean = s * (1.f / D_);
  float d = (cnt > 0.f) ? (pp.x / 48.f - mean) : 0.f;
  float m2 = (cnt > 0.f) ? (pp.y + cnt * d * d) : 0.f;
#pragma unroll
  for (int o = 8; o >= 1; o >>= 1) m2 += __shfl_xor(m2, o);
  const float a = rsqrtf(m2 * (1.f / D_) + 1e-5f), b = -mean * a;
  const float* sb = mod + 3 * D_;
  const float* cb = mod + 4 * D_;
  for (int c = lane; c < 72; c += 64) {
    u32x4 r = *reinterpret_cast<const u32x4*>(x1 + (size_t)row * D_ + c * 8);
    f32x4 sc0 = *reinterpret_cast<const f32x4*>(cb + c * 8), sc1 = *reinterpret_cast<const f32x4*>(cb + c * 8 + 4);
    f32x4 sh0 = *reinterpret_cast<const f32x4*>(sb + c * 8), sh1 = *reinterpret_cast<const f32x4*>(sb + c * 8 + 4);
    u32x4 o;
    o[0] = pack2((bflo(r[0]) * a + b) * (sc0[0] + 1.f) + sh0[0], (bfhi(r[0]) * a + b) * (sc0[1] + 1.f) + sh0[1]);
    o[1] = pack2((bflo(r[1]) * a + b) * (sc0[2] + 1.f) + sh0[2], (bfhi(r[1]) * a + b) * (sc0[3] + 1.f) + sh0[3]);
    o[2] = pack2((bflo(r[2]) * a + b) * (sc1[0] + 1.f) + sh1[0], (bfhi(r[2]) * a + b) * (sc1[1] + 1.f) + sh1[1]);
    o[3] = pack2((bflo(r[3]) * a + b) * (sc1[2] + 1.f) + sh1[2], (bfhi(r[3]) * a + b) * (sc1[3] + 1.f) + sh1[3]);
    *reinterpret_cast<u32x4*>(h2 + (size_t)row * D_ + c * 8) = o;
  }
}

struct EpiGLU {
  u16* hh; int m0, n0;
  template <int MT, int NTW, int NTHR>
  DI void run(f32x4 (&acc)[MT][NTW], char* lds, int row0, int tile0, int ntw, int fq) const {
    constexpr int RS = 592;
#pragma unroll
    for (int m = 0; m < MT; ++m)
#pragma unroll
      for (int n = 0; n < NTW; ++n) {
        u32x2 o;
        o[0] = pack2(acc[m][n][0], acc[m][n][1]);
        o[1] = pack2(acc[m][n][2], acc[m][n][3]);
        *reinterpret_cast<u32x2*>(lds + (row0 + m * 16) * RS + ((tile0 + n) * 16 + fq * 4) * 2) = o;
      }
    __syncthreads();
    for (int it = threadIdx.x; it < 256 * 18; it += NTHR) {
      const int row = it / 18, c = it % 18;
      const u32x4 g = *reinterpret_cast<const u32x4*>(lds + row * RS + c * 16);
      const u32x4 u = *reinterpret_cast<const u32x4*>(lds + row * RS + 288 + c * 16);
      u32x4 w;
#pragma unroll
      for (int j = 0; j < 4; ++j) w[j] = pack2(bflo(g[j]) * bflo(u[j]), bfhi(g[j]) * bfhi(u[j]));
      *reinterpret_cast<u32x4*>(hh + (size_t)(m0 + row) * F_ + n0 + c * 8) = w;
    }
  }
};

constexpr int LDS_GLU = 256 * 592;
__global__ __launch_bounds__(512) void k_glu(const u16* __restrict__ h2, const u16* __restrict__ wg,
                                              const u16* __restrict__ wu, u16* __restrict__ hh) {
  extern __shared__ __attribute__((aligned(16))) char lds[];
  const int b = blockIdx.x, xcd = b & 7, idx = b >> 3;
  const int bm = idx & 7, bn = xcd * 4 + (idx >> 3);
  const int n0 = bn * 144;
  EpiGLU epi{hh, bm * 256, n0};
  gemm_body<256, 288, 2, 4, 4, 2, 32, 4, 0>(lds, h2, D_, bm * 256, 0, D_ / 32, nullptr, nullptr, nullptr, wg + n0, wu + n0, F_, epi);
}

struct EpiPart {
  float* part; int m0, n0;
  template <int MT, int NTW, int NTHR>
  DI void run(f32x4 (&acc)[MT][NTW], char* lds, int row0, int tile0, int ntw, int fq) const {
    constexpr int RS = 592;
#pragma unroll
    for (int m = 0; m < MT; ++m)
#pragma unroll
      for (int n = 0; n < NTW; ++n)
        if (n < ntw) *reinterpret_cast<f32x4*>(lds + (row0 + m * 16) * RS + ((tile0 + n) * 16 + fq * 4) * 4) = acc[m][n];
    __syncthreads();
    for (int it = threadIdx.x; it < 128 * 36; it += NTHR) {
      const int row = it / 36, c = it % 36;
      f32x4 w = *reinterpret_cast<const f32x4*>(lds + row * RS + c * 16);
      *reinterpret_cast<f32x4*>(part + (size_t)(m0 + row) * D_ + n0 + c * 4) = w;
    }
  }
};

constexpr int LDS_DOWN = 4 * (128 * 64 * 2 + 64 * 144 * 2);
__global__ __launch_bounds__(512) void k_down(const u16* __restrict__ hh, const u16* __restrict__ wd,
                                               float* __restrict__ part) {
  extern __shared__ __attribute__((aligned(16))) char lds[];
  const int b = blockIdx.x, xcd = b & 7, idx = b >> 3;
  const int sk = xcd >> 1, bn = idx & 3, bm = (xcd & 1) * 8 + (idx >> 2);
  const int n0 = bn * 144;
  EpiPart epi{part + (size_t)sk * S_ * D_, bm * 128, n0};
  gemm_body<128, 144, 1, 2, 4, 2, 64, 4, 0>(lds, hh, F_, bm * 128, sk * (F_ / 4), F_ / 4 / 64, nullptr, nullptr, nullptr, wd + n0, wd + n0, D_, epi);
}

__global__ __launch_bounds__(256) void k_final(const float* __restrict__ part, const u16* __restrict__ x1,
                                                const float* __restrict__ mod, u16* __restrict__ out) {
  const int i = (blockIdx.x * 256 + threadIdx.x) * 8;
  if (i >= S_ * D_) return;
  const int col = i % D_;
  const float* gb = mod + 5 * D_ + col;
  u32x4 xv = *reinterpret_cast<const u32x4*>(x1 + i);
  u32x4 o;
#pragma unroll
  for (int j = 0; j < 4; ++j) {
    float a = 0.f, b = 0.f;
#pragma unroll
    for (int s = 0; s < 4; ++s) {
      f32x2 p = *reinterpret_cast<const f32x2*>(part + (size_t)s * S_ * D_ + i + 2 * j);
      a += p[0]; b += p[1];
    }
    o[j] = pack2(bflo(xv[j]) + gb[2 * j] * a, bfhi(xv[j]) + gb[2 * j + 1] * b);
  }
  *reinterpret_cast<u32x4*>(out + i) = o;
}

extern "C" void kernel_launch(void* const* d_in, const int* in_sizes, int n_in,
                              void* d_out, int out_size, void* d_ws, size_t ws_size,
                              hipStream_t stream) {
  const u16* x = (const u16*)d_in[0];
  const u16* wqkv = (const u16*)d_in[1];
  const u16* wo = (const u16*)d_in[2];
  const u16* wg = (const u16*)d_in[4];
  const u16* wu = (const u16*)d_in[5];
  const u16* wd = (const u16*)d_in[6];
  const u16* c = (const u16*)d_in[7];
  const u16* wmod = (const u16*)d_in[8];
  char* ws = (char*)d_ws;
  float* mod = (float*)(ws + WS_MOD);
  float2* st1 = (float2*)(ws + WS_ST1);
  float2* st2 = (float2*)(ws + WS_ST2);
  u16* Q = (u16*)(ws + WS_Q);
  u16* K = (u16*)(ws + WS_K);
  u16* V = (u16*)(ws + WS_V);
  u16* O = (u16*)(ws + WS_O);
  u16* x1 = (u16*)(ws + WS_X1);
  u16* h2 = (u16*)(ws + WS_H2);
  u16* hh = (u16*)(ws + WS_HH);
  float* part = (float*)(ws + WS_PART);

  (void)hipFuncSetAttribute((const void*)k_qkv, hipFuncAttributeMaxDynamicSharedMemorySize, LDS_QKV);
  (void)hipFuncSetAttribute((const void*)k_proj, hipFuncAttributeMaxDynamicSharedMemorySize, LDS_PROJ);
  (void)hipFuncSetAttribute((const void*)k_glu, hipFuncAttributeMaxDynamicSharedMemorySize, LDS_GLU);
  (void)hipFuncSetAttribute((const void*)k_down, hipFuncAttributeMaxDynamicSharedMemorySize, LDS_DOWN);

  k_prep<<<108 + 512, 256, 0, stream>>>(c, wmod, x, mod, st1);
  k_qkv<<<192, 512, LDS_QKV, stream>>>(x, wqkv, st1, mod, Q);
  k_attn<<<32 * 16, 512, 0, stream>>>(Q, K, V, O, wg, wu, wd);
  k_proj<<<256, 384, LDS_PROJ, stream>>>(O, wo, x, mod, x1, st2);
  k_ln2<<<512, 256, 0, stream>>>(x1, st2, mod, h2);
  k_glu<<<256, 512, LDS_GLU, stream>>>(h2, wg, wu, hh);
  k_down<<<256, 512, LDS_DOWN, stream>>>(hh, wd, part);
  k_final<<<(S_ * D_ / 8 + 255) / 256, 256, 0, stream>>>(part, x1, mod, (u16*)d_out);
}
```
